```python
import torch

def _softmax(x, axis=-1):
    return torch.softmax(x, dim=axis)
def _gelu(x):
    return torch.nn.functional.gelu(x, approximate="tanh")
def _silu(x):
    return torch.nn.functional.silu(x)
def _sigmoid(x):
    return torch.sigmoid(x)
def _layernorm(x, axis=-1, eps=1e-5):
    m = x.mean(dim=axis, keepdim=True)
    v = x.var(dim=axis, keepdim=True, unbiased=False)
    return (x - m) / torch.sqrt(v + eps)
def _scatter_add(tgt, idx, src, axis=0):
    return tgt.clone().index_add_(axis, idx, src)
def _segment_sum(src, seg, n):
    out = torch.zeros((n,) + src.shape[1:], dtype=src.dtype)
    return out.index_add_(0, seg, src)
def _pad(x, pw):
    flat = tuple(v for pair in reversed(pw) for v in pair)
    return torch.nn.functional.pad(x, flat)

kernel_name = 'interleave_layers__linear_attn_block__mixer_token_mlp__prompt'

def setup_inputs():

    _w = (lambda a: (lambda t: t.to(torch.bfloat16) if t.is_floating_point() else t)(torch.as_tensor(a)))
    def _A_setup():
        B, S, D = 1, 2048, 448
        return {
            "x": _w((lambda *s: torch.randn(*s, dtype=torch.bfloat16) * 0.05)(B, S, D)),
            "wqkv": _w((lambda *s: torch.randn(*s, dtype=torch.bfloat16) * 0.1)(D, 3 * D)),
            "wo": _w((lambda *s: torch.randn(*s, dtype=torch.bfloat16) * 0.05)(D, D)),
        }
    _A_in = _A_setup()

    def _B_setup():
        B, S, D, F = 1, 2048, 448, 1792
        return {
            "x": _w((lambda *s: torch.randn(*s, dtype=torch.bfloat16) * 0.05)(B, S, D)),
            "w1": _w((lambda *s: torch.randn(*s, dtype=torch.bfloat16) * 0.8)(S, F) * (S ** -0.5)),
            "b1": _w((lambda *s: torch.randn(*s, dtype=torch.bfloat16) * 12.8)(F) * 0.02),
            "w2": _w((lambda *s: torch.randn(*s, dtype=torch.bfloat16) * 0.4)(F, S) * (F ** -0.5)),
            "b2": _w((lambda *s: torch.randn(*s, dtype=torch.bfloat16) * 6.4)(S) * 0.02),
        }
    _B_in = _B_setup()

    return {
        "A_x": _A_in["x"],
        "A_wqkv": _A_in["wqkv"],
        "A_wo": _A_in["wo"],
        "B_x": _B_in["x"],
        "B_w1": _B_in["w1"],
        "B_b1": _B_in["b1"],
        "B_w2": _B_in["w2"],
        "B_b2": _B_in["b2"],
    }

def reference(A_x, A_wqkv, A_wo, B_x, B_w1, B_b1, B_w2, B_b2):
    def _A_ref(x, wqkv, wo):
        B, S, D = x.shape
        H = 16; Dh = D // H
        qkv = (lambda x, *d: x.permute(*d))((x @ wqkv).reshape(B, S, 3, H, Dh), 2, 0, 3, 1, 4)
        q, k, v = qkv[0], qkv[1], qkv[2]
        phi = lambda t: torch.where(t > 0, t + 1, torch.exp(t)) + 1e-6
        pq, pk = phi(q), phi(k)
        kv = torch.einsum("bhsd,bhse->bhde", pk, v)
        z = torch.einsum("bhsd->bhd", pk)
        num = torch.einsum("bhqd,bhde->bhqe", pq, kv)
        den = torch.einsum("bhqd,bhd->bhq", pq, z)[..., None] + 1e-6
        o = num / den
        return (lambda x, *d: x.permute(*d))(o, 0, 2, 1, 3).reshape(B, S, D) @ wo

    def _B_ref(x, w1, b1, w2, b2):
        B, S, D = x.shape
        h = _layernorm(x, axis=-1)
        u = torch.einsum("bsd,sf->bdf", h, w1) + b1
        u = _gelu(u)
        y = torch.einsum("bdf,fs->bds", u, w2) + b2
        return (lambda x, *d: x.permute(*d))(y, 0, 2, 1)

    x = A_x
    x = x + _A_ref(_layernorm(x, axis=-1), A_wqkv, A_wo)
    x = x + _B_ref(_layernorm(x, axis=-1), B_w1, B_b1, B_w2, B_b2)
    x = x + _A_ref(_layernorm(x, axis=-1), A_wqkv, A_wo)
    x = x + _B_ref(_layernorm(x, axis=-1), B_w1, B_b1, B_w2, B_b2)
    return x
```

```cpp
#include <hip/hip_runtime.h>

typedef unsigned short u16;
typedef unsigned long long u64;
typedef unsigned v4u __attribute__((ext_vector_type(4)));
typedef unsigned v2u __attribute__((ext_vector_type(2)));
typedef float f32x4 __attribute__((ext_vector_type(4)));
typedef float f32x2 __attribute__((ext_vector_type(2)));
typedef unsigned v8u __attribute__((ext_vector_type(8)));
typedef unsigned v16u __attribute__((ext_vector_type(16)));
typedef unsigned v32u __attribute__((ext_vector_type(32)));
typedef short v4s __attribute__((ext_vector_type(4)));
typedef __bf16 bf16x8 __attribute__((ext_vector_type(8)));
typedef __bf16 bf16x2 __attribute__((ext_vector_type(2)));
typedef __amdgpu_buffer_rsrc_t rsrc_t;
#define LAS __attribute__((address_space(3)))
#define DEV __device__ __forceinline__

constexpr unsigned MBS = 256;
constexpr unsigned OFF_MB = 4096;
constexpr unsigned MB_HOP = 1024 * MBS, MB_L = 8 * MB_HOP;
enum { HA = 0, HB, HC, HD, HE, HF, HW1, HW2 };
constexpr unsigned OFF_STAT = OFF_MB + 2 * MB_L;
constexpr unsigned STAT_L = 128 * 8 * 128;
constexpr unsigned OFF_KVP = OFF_STAT + 2 * STAT_L;
constexpr unsigned KVP_L = 1024 * 1024;
constexpr unsigned OFF_O = OFF_KVP + 2 * KVP_L;
constexpr unsigned O_L = 2048 * 1024;
constexpr unsigned OFF_H = OFF_O + 2 * O_L;
constexpr unsigned H_L = 7 * 64 * 4 * 1024;
constexpr unsigned OFF_U = OFF_H + 2 * H_L;
constexpr unsigned U_L = 7 * 56 * 4 * 1024;
constexpr unsigned OFF_X2 = OFF_U + 2 * U_L;
constexpr unsigned X_L = 128 * 14 * 1024;
constexpr unsigned OFF_WQI = OFF_X2 + 2 * X_L;
constexpr unsigned OFF_DBG = OFF_WQI + 64 * 21 * 1024;
constexpr unsigned WS_BYTES = OFF_DBG + 256 * 1024;
constexpr unsigned L_WQ = 0;
constexpr unsigned L_R = 86016;
constexpr unsigned L_M = L_R + 57344;
constexpr unsigned L_KVR = L_M;
constexpr unsigned L_HT = L_R;
constexpr unsigned L_XS = L_M;
constexpr unsigned L_Z = L_R + 24576;
constexpr unsigned L_KVF = L_M + 16384;
constexpr unsigned L_CS = L_M + 18432;
constexpr unsigned L_CSQ = L_CS + 256;
constexpr unsigned L_B1 = L_CS + 1792;
constexpr unsigned L_JUNK = L_CS + 512;
constexpr unsigned L_B2 = L_CS + 1536;
constexpr unsigned LDS_BYTES = 163840;
constexpr unsigned OOB = 0x80000000u;
constexpr unsigned SPIN_CAP = 1u << 15;
constexpr int SC1 = 16;
#define CODE_BYTES 90112

DEV f32x4 mfma(v4u a, v4u b, f32x4 c) {
  return __builtin_amdgcn_mfma_f32_16x16x32_bf16(__builtin_bit_cast(bf16x8, a), __builtin_bit_cast(bf16x8, b), c, 0, 0, 0);
}
DEV f32x4 mfma_v(v4u a, v4u b) {
  f32x4 d;
  asm volatile("v_mfma_f32_16x16x32_bf16 %0, %1, %2, 0" : "=&v"(d) : "v"(a), "v"(b));
  return d;
}
DEV void mfma_va(f32x4& d, v4u a, v4u b) { asm volatile("v_mfma_f32_16x16x32_bf16 %0, %1, %2, %0" : "+v"(d) : "v"(a), "v"(b)); }
DEV unsigned pk(float lo, float hi) {
  f32x2 f = {lo, hi};
  return __builtin_bit_cast(unsigned, __builtin_convertvector(f, bf16x2));
}
DEV float asf(unsigned u) { return __builtin_bit_cast(float, u); }
DEV unsigned asu(float f) { return __builtin_bit_cast(unsigned, f); }
DEV float bflo(unsigned u) { return asf(u << 16); }
DEV float bfhi(unsigned u) { return asf(u & 0xffff0000u); }
DEV rsrc_t mk(const void* p, unsigned bytes) { return __builtin_amdgcn_make_buffer_rsrc((void*)p, 0, bytes, 0x00020000); }
DEV v4u ld16(rsrc_t r, unsigned v, unsigned s) { return __builtin_amdgcn_raw_buffer_load_b128(r, v, s, 0); }
DEV v4u ld16c(rsrc_t r, unsigned v, unsigned s) { return __builtin_amdgcn_raw_buffer_load_b128(r, v, s, SC1); }
DEV void st16c(v4u d, rsrc_t r, unsigned v, unsigned s) { __builtin_amdgcn_raw_buffer_store_b128(d, r, v + s, 0, SC1); }
#define AUXA 16
#define AUXW 16
DEV v4u ld16a(rsrc_t r, unsigned v, unsigned s) { return __builtin_amdgcn_raw_buffer_load_b128(r, v, s, AUXA); }
DEV v4u ld16w(rsrc_t r, unsigned v, unsigned s) { return __builtin_amdgcn_raw_buffer_load_b128(r, v, s, AUXW); }
DEV void st8c(v2u d, rsrc_t r, unsigned v, unsigned s) { __builtin_amdgcn_raw_buffer_store_b64(d, r, v, s, SC1); }
DEV void st4c(unsigned d, rsrc_t r, unsigned v, unsigned s) { __builtin_amdgcn_raw_buffer_store_b32(d, r, v, s, SC1); }
DEV void dma16(rsrc_t r, unsigned v, unsigned s, unsigned lds) {
  asm volatile("s_mov_b32 m0, %3\n\ts_nop 0\n\tbuffer_load_dwordx4 %0, %1, %2 offen lds" ::"v"(v), "s"(r), "s"(s), "s"(lds) : "memory");
}
DEV void dma16c(rsrc_t r, unsigned v, unsigned s, unsigned lds) {
  asm volatile("s_mov_b32 m0, %3\n\ts_nop 0\n\tbuffer_load_dwordx4 %0, %1, %2 offen sc1 lds" ::"v"(v), "s"(r), "s"(s), "s"(lds) : "memory");
}
DEV void dma4(rsrc_t r, unsigned v, unsigned s, unsigned lds) {
  asm volatile("s_mov_b32 m0, %3\n\ts_nop 0\n\tbuffer_load_dword %0, %1, %2 offen lds" ::"v"(v), "s"(r), "s"(s), "s"(lds) : "memory");
}
DEV void ld16h(v4u& d, rsrc_t r, unsigned v, unsigned s) {
  asm volatile("buffer_load_dwordx4 %0, %1, %2, %3 offen" : "=v"(d) : "v"(v), "s"(r), "s"(s) : "memory");
}
template <int O> DEV void ld16hc(v4u& d, rsrc_t r, unsigned v, unsigned s) {
  asm volatile("buffer_load_dwordx4 %0, %1, %2, %3 offen offset:%4 sc1" : "=v"(d) : "v"(v), "s"(r), "s"(s), "n"(O) : "memory");
}
template <int N> DEV void ldw1(v4u& a) { asm volatile("s_waitcnt vmcnt(%1)" : "+v"(a) : "n"(N) : "memory"); }
template <int N> DEV void ldw(v4u& a, v4u& b, v4u& c, v4u& d) {
  asm volatile("s_waitcnt vmcnt(%4)" : "+v"(a), "+v"(b), "+v"(c), "+v"(d) : "n"(N) : "memory");
}
DEV v2u trd(unsigned a) { return __builtin_bit_cast(v2u, __builtin_amdgcn_ds_read_tr16_b64_v4i16((LAS v4s*)a)); }
DEV v4u cat(v2u a, v2u b) { return v4u{a.x, a.y, b.x, b.y}; }
DEV v4u trf(unsigned a, unsigned b) { return cat(trd(a), trd(b)); }
template <class T> DEV T lds_ld(unsigned a) { return *(LAS T*)a; }
template <class T> DEV void lds_st(unsigned a, T v) { *(LAS T*)a = v; }
#define BAR() asm volatile("s_waitcnt lgkmcnt(0)\n\ts_barrier" ::: "memory")
#define VMW(n) asm volatile("s_waitcnt vmcnt(" #n ")" ::: "memory")
#define OPQ(x) asm volatile("" : "+s"(x))
#define OPV(x) asm volatile("" : "+v"(x))
#define SB() __builtin_amdgcn_sched_barrier(0)
#define NR 8
#define KEEP() do { v32u r0_, r1_, r2_, r3_; asm volatile("" : "={a[128:159]}"(r3_), "={a[160:191]}"(r0_), "={a[192:223]}"(r1_), "={a[224:255]}"(r2_)); SB(); } while (0)
#define FENCE() do { asm volatile("" ::: "memory"); KEEP(); } while (0)
DEV float xsum16_32(float x) {
  unsigned u = asu(x);
  auto a = __builtin_amdgcn_permlane16_swap(u, u, false, false);
  u = asu(asf(a[0]) + asf(a[1]));
  auto b = __builtin_amdgcn_permlane32_swap(u, u, false, false);
  return asf(b[0]) + asf(b[1]);
}
DEV float bperm(unsigned src4, float x) { return asf(__builtin_amdgcn_ds_bpermute(src4, asu(x))); }
DEV f32x2 fma2(f32x2 a, f32x2 b, f32x2 c) { return __builtin_elementwise_fma(a, b, c); }
DEV f32x2 bc(float x) { return f32x2{x, x}; }
DEV f32x2 lo2(f32x4 v) { return f32x2{v[0], v[1]}; }
DEV f32x2 hi2(f32x4 v) { return f32x2{v[2], v[3]}; }
DEV f32x2 half2(f32x4 v, int p) { return p ? hi2(v) : lo2(v); }
DEV unsigned pk2(f32x2 f) { return __builtin_bit_cast(unsigned, __builtin_convertvector(f, bf16x2)); }
DEV f32x2 up2(unsigned u) { return f32x2{bflo(u), bfhi(u)}; }
DEV f32x4 up4(v2u u) { return f32x4{bflo(u.x), bfhi(u.x), bflo(u.y), bfhi(u.y)}; }
DEV float dot2(unsigned a, unsigned b, float c) {
  return __builtin_amdgcn_fdot2_f32_bf16(__builtin_bit_cast(bf16x2, a), __builtin_bit_cast(bf16x2, b), c, false);
}
constexpr float LOG2E = 1.4426950408889634f;
DEV bool anynan(float a, float b) { return __builtin_amdgcn_ballot_w64(__builtin_isunordered(a, b)) != 0; }

#define STAMP(n)

constexpr __host__ __device__ unsigned sl16(unsigned s) { return (s >> 3) * 64 + (s & 7) * 4; }
constexpr __host__ __device__ unsigned sl8(unsigned s) { return (s >> 2) * 64 + (s & 3) * 4; }
#define LD16(B, O) "s_load_dwordx8 s[80:87], " B ", " O " glc\n\ts_load_dwordx8 s[88:95], " B ", " O " offset:64 glc"
#define LD8(B, O) "s_load_dwordx4 s[80:83], " B ", " O " glc\n\ts_load_dwordx4 s[84:87], " B ", " O " offset:64 glc"
template <int N> struct MbT { typedef v16u type; };
template <> struct MbT<8> { typedef v8u type; };
template <> struct MbT<7> { typedef v8u type; };
template <int N> DEV typename MbT<N>::type peek(const char* base, unsigned off) {
  typename MbT<N>::type m;
  if constexpr (N > 8) asm volatile(LD16("%1", "%2") : "={s[80:95]}"(m) : "s"(base), "s"(off) : "memory");
  else asm volatile(LD8("%1", "%2") : "={s[80:87]}"(m) : "s"(base), "s"(off) : "memory");
  return m;
}
#define XR(a, b) "s_xor_b64 s[" #a ":" #b "], s[" #a ":" #b "], %4\n\t"
#define OR(a, b, c, d) "s_or_b64 s[" #a ":" #b "], s[" #a ":" #b "], s[" #c ":" #d "]\n\t"
#define POLL_HEAD "1:\n\ts_waitcnt lgkmcnt(0)\n\t"
#define POLL_TAIL(LD) "s_cbranch_scc0 2f\n\ts_cmp_eq_u32 %5, 0\n\ts_cbranch_scc1 2f\n\ts_cmp_eq_u32 %0, 0\n\ts_cbranch_scc1 2f\n\t" \
                      LD("%2", "%3") "\n\ts_sub_u32 %0, %0, 1\n\ts_branch 1b\n\t2:"
#define POLL_OPS(R) : "+s"(budget), "+{" R "}"(m) : "s"(base), "s"(off), "s"(ee), "s"(on) : "memory", "scc"
template <int N> DEV void poll(typename MbT<N>::type m, const char* base, unsigned off, u64 ee, unsigned on, unsigned& budget) {
  if constexpr (N == 16)
    asm volatile(POLL_HEAD XR(80, 81) XR(82, 83) XR(84, 85) XR(86, 87) XR(88, 89) XR(90, 91) XR(92, 93) XR(94, 95)
                 OR(80, 81, 82, 83) OR(84, 85, 86, 87) OR(88, 89, 90, 91) OR(92, 93, 94, 95)
                 OR(80, 81, 84, 85) OR(88, 89, 92, 93) OR(80, 81, 88, 89) POLL_TAIL(LD16) POLL_OPS("s[80:95]"));
  else if constexpr (N == 14)
    asm volatile(POLL_HEAD "s_mov_b32 s87, s94\n\t" XR(80, 81) XR(82, 83) XR(84, 85) XR(86, 87) XR(88, 89) XR(90, 91) XR(92, 93)
                 OR(80, 81, 82, 83) OR(84, 85, 86, 87) OR(88, 89, 90, 91)
                 OR(80, 81, 84, 85) OR(88, 89, 92, 93) OR(80, 81, 88, 89) POLL_TAIL(LD16) POLL_OPS("s[80:95]"));
  else if constexpr (N == 8)
    asm volatile(POLL_HEAD XR(80, 81) XR(82, 83) XR(84, 85) XR(86, 87) OR(80, 81, 82, 83) OR(84, 85, 86, 87) OR(80, 81, 84, 85)
                 POLL_TAIL(LD8) POLL_OPS("s[80:87]"));
  else
    asm volatile(POLL_HEAD "s_mov_b32 s87, s86\n\t" XR(80, 81) XR(82, 83) XR(84, 85) XR(86, 87) OR(80, 81, 82, 83) OR(84, 85, 86, 87)
                 OR(80, 81, 84, 85) POLL_TAIL(LD8) POLL_OPS("s[80:87]"));
}
#define WAITMB(N, off, on) do { const unsigned o__ = (off); poll<N>(peek<N>(gws, o__), gws, o__, EE, on, budget); } while (0)
template <int N> DEV void ld_settled(v4u (&x)[N], rsrc_t rs, unsigned v, unsigned s, unsigned st) {
#pragma nounroll
  for (unsigned left = 4096;; --left) {
    unsigned m = 0;
#pragma unroll
    for (int i = 0; i < N; ++i) x[i] = ld16c(rs, v, s + i * st);
    asm volatile("" ::: "memory");
#pragma unroll
    for (int i = 0; i < N; ++i) m = max(max(max(m, x[i].x), max(x[i].y, x[i].z)), x[i].w);
    if (__builtin_amdgcn_ballot_w64(m == ~0u) == 0 || left == 0) break;
  }
}
DEV void poll_stat(rsrc_t rs, unsigned v0, unsigned v1, unsigned soff, v2u& s0, v2u& s1) {
  u64 m64;
  unsigned budget = 4096;
  asm volatile(
      "1:\n\t"
      "buffer_load_dwordx2 %0, %4, %6, %7 offen sc1\n\t"
      "buffer_load_dwordx2 %1, %5, %6, %7 offen sc1\n\t"
      "s_sub_u32 %3, %3, 1\n\t"
      "s_cselect_b32 %3, 0, %3\n\t"
      "s_waitcnt vmcnt(0)\n\t"
      "v_cmp_eq_u64 vcc, -1, %0\n\t"
      "v_cmp_eq_u64 %2, -1, %1\n\t"
      "s_cbranch_scc1 2f\n\t"
      "s_or_b64 vcc, vcc, %2\n\t"
      "s_cbranch_vccnz 1b\n\t"
      "2:"
      : "=&v"(s0), "=&v"(s1), "=&s"(m64), "+s"(budget)
      : "v"(v0), "v"(v1), "s"(rs), "s"(soff)
      : "memory", "vcc", "scc");
}
#define REDO_COUNT(n)
template <int I> struct IC { static constexpr int v = I; };
template <int I, int N, int K = N - 2, class F> DEV void static_for(F&& f) {
  if constexpr (I < N) { f(IC<I>{}); if constexpr (I < K && I % 2 == 0) KEEP(); static_for<I + 1, N, K>(f); }
}
DEV unsigned umax_(unsigned a, unsigned b) { return a > b ? a : b; }
DEV unsigned umin_(unsigned a, unsigned b) { return a < b ? a : b; }
DEV unsigned oob_unless(bool c, unsigned v) { OPV(v); return c ? v : OOB; }
DEV float sel(bool c, float a, float b) { return c ? a : b; }

#define INL __attribute__((always_inline))
#define W2PLAIN 0
#define RD 12
#define NRES 24
constexpr unsigned slot_at(int s) { return s < 3 ? L_WQ + s * 16384 : L_Z + (s - 3) * 16384; }
constexpr int NPRE = 14, PRE_SLOT[NPRE] = {0, 1, 2, 5, 0, 1, 2, 5, 0, 1, 3, 4, 2, 5};
constexpr int u0(int L) { return L ? 0 : 3; }
constexpr int nu1(int L) { return L ? 16 - NR : 16; }
#define WOP0 11
#define WOP1 10
#define PKF1 28
#define PLF1 61
#define XB1 8
#define WFP 7
#define PKE 5
#define RT0 1
#define WARM 7
#define WOG 2
#define PKI 7
#define PINA 0
#define RAWA0 42
#define RAWA1 18
#define RAWV0 0
#define RAWV1 0
#define PKA1 2
#define QE1 2
#define PKA0 2
#define PKB 0
#define PKC 0
#define PKD 3
#define CHKD 14
#define FB_AT 1
#define FA_AT 1
#define FC_AT 12
#define FD_AT 0
#define FE_AT 3
#define FF_AT 0
constexpr int gn(int i) { return i < 64 ? i & 31 : (i - 64) % 28; }
constexpr int gh(int i) { return i < 64 ? i >> 5 : (i - 64) / 28; }
constexpr int gr(int i) { return i < 64 ? 24 : 20; }
constexpr int gk(int i) { return gn(i) < gr(i) ? gn(i) >> 1 : gr(i) / 2 + ((gn(i) - gr(i)) & 3); }
constexpr int gc(int i) { return gn(i) < gr(i) ? gn(i) & 1 : (gn(i) - gr(i)) >> 2; }
#define IMG0 87
#define MBWARM 1
#define MBAUX "sc1 nt"
#define RDS 1
#define CHKE 4
#define WS0 24
#define WS1 44
#define FE0 45
#define FE1 73
#define PE0 52
#define PE1 80
#define PX0 111
constexpr int B0 = 35;
constexpr int imin(int a, int b) { return a < b ? a : b; }
constexpr int cum(int i) {
  return i <= B0 ? i + RD + 1 : i < PE0 ? imin(64, i + RD + 5) : i < PE1 ? imin(92, imin(64 + 2 * (i - PE0 + 1), i + RD + 1))
         : i < PX0 ? imin(120, imin(92 + 2 * (i - PE1 + 1), i + RD + 1)) : imin(134, 120 + 2 * (i - PX0 + 1));
}
constexpr int cum0(int i) { return i == B0 + 1 ? cum(B0) + 4 : i ? cum(i - 1) : RD; }
static_assert(cum(PE0 - 1) == 64 && cum(PE1 - 1) == 92 && cum(PX0 - 1) == 120, "");
#define NY 10
#define NEARLY 3
struct WPlan { int dma[120][6]; int rd[121]; int wait[120]; int slot[30]; int done[30]; int last; };
constexpr WPlan w_plan(int L, int ws) {
  WPlan p{};
  const int nu = nu1(L) + 14;
  int cnt = 0, k = 4 * (u0(L) + NPRE), r = u0(L) + NPRE - 4, issued[30] = {}, holder[6] = {-1, -1, -1, -1, -1, -1};
  for (int u = 0; u < 30; ++u) { p.slot[u] = u < u0(L) ? 3 + u : u < u0(L) + NPRE ? PRE_SLOT[u - u0(L)] : -1; p.done[u] = u < r ? -9 : 999; issued[u] = -99; }
  for (int u = r; u < u0(L) + NPRE; ++u) holder[p.slot[u]] = u;
  for (int i = 0; i < 121; ++i) p.rd[i] = -1;
  for (int i = 0; i < 120; ++i) {
    p.wait[i] = 0;
    for (int z = 0; z < 6; ++z) p.dma[i][z] = -1;
    if (p.rd[i] < 0 && i >= RDS && r < nu && 4 * r + 4 <= k && cnt - issued[r] >= NY && (i < 32 || gn(i) > 2) && gn(i + 1) > 0) {
      const int e = r - nu1(L) - NEARLY;
      if (e < 0 || i >= B0 + 1 + 2 * e) { p.rd[i] = 2 * r; p.rd[i + 1] = 2 * r + 1; p.wait[i] = issued[r] >= 0; p.done[r] = i + 1; ++r; }
    }
    const int used = cum(i) - cum0(i);
    for (int z = 0; z < 6; ++z) {
      const bool place = z == 0 ? used == 0 : z == 1 ? used < 2 && i >= ws : i == 67;
      if (z < 2 && used > z) ++cnt;
      if (!place || k >= 4 * nu) continue;
      if (k % 4 == 0) {
        int f = -1;
        for (int y = 5; y >= 0; --y) if (f < 0 && (holder[y] < 0 || p.done[holder[y] + 1] < i)) f = y;
        if (f < 0) continue;
        holder[f] = k / 4; p.slot[k / 4] = f;
      }
      p.dma[i][z] = k; ++cnt; issued[k / 4] = cnt; ++k;
    }
    if (i == B0) cnt += 4;
  }
  p.last = p.done[nu - 1];
  return p;
}
constexpr int RK0 = 14 - NR, RA0 = 256 - 16 * NR;
constexpr bool res_k(int k) { return k >= RK0 && k < 14; }
constexpr int w1k(int L, int u) { return L == 0 && u < NR ? RK0 + u : (u - (L ? 0 : NR)) < RK0 ? u - (L ? 0 : NR) : u + (L ? NR : 0); }
constexpr int w1u(int L, int k) { return res_k(k) ? (L ? -1 : k - RK0) : (k < RK0 ? k : k - NR) + (L ? 0 : NR); }
constexpr WPlan WP[2] = {w_plan(0, WS0), w_plan(1, WS1)};
#define WD0 0
#define CODE1 1
#define TW 7
#define TAP 1
#define TCP 0
#define MKP 0
#define TB 28
#define QS0 8
#define MK0 6
#define XI0 12
#define WO0 0
#define WOS 25
constexpr int p1_wo(int n) {
  const int e = (PLF1 - WOS + 4) / 5;
  return n >= WOS && n < PLF1 && (n - WOS) % 5 == 0 ? (n - WOS) / 5 : n >= 93 && (n - 93) % 6 == 0 && e + (n - 93) / 6 < 14 ? e + (n - 93) / 6 : -1;
}
static_assert((PLF1 - WOS + 4) / 5 + 8 >= 14, "");
constexpr int p1_younger(int L, bool idle, int n, int k) {
  if (L == 0) return imin(XI0 + (n + 3) / 4, 49 - WO0) - k - 1 + ((TW & 2) && XI0 + (n + 3) / 4 > TB);
  int c = 14 + (69 - PLF1) / 4 + 1 + 7;
  for (int i = 70; i < n; ++i) c += (i % 4 == 0 && c < 28) + (i >= 93 && !idle && p1_wo(i) >= 0);
  return c - k - 1;
}
constexpr int pm(int i) { return i < 7 ? 2 * i : 2 * (i - 7) + 1; }
#define LANE_COPY() unsigned ln = l16 >> 4; OPV(ln); const unsigned li = ln & 15, g = ln >> 4; (void)li; (void)g
#define UNLIKELY(x) __builtin_expect(!!(x), 0)

__global__ void __launch_bounds__(256, 1)
fused_kernel(const u16* __restrict__ gx, const u16* __restrict__ gwqkv, const u16* __restrict__ gwo, const u16* __restrict__ gw1,
             const u16* __restrict__ gb1, const u16* __restrict__ gw2, const u16* __restrict__ gb2, u16* gout, char* gws) {
  __shared__ __attribute__((aligned(16))) char smem[LDS_BYTES];
  asm volatile("" ::"v"((unsigned)(size_t)(LAS char*)smem) : "memory");
  const unsigned lane = threadIdx.x & 63;
  unsigned w = __builtin_amdgcn_readfirstlane(threadIdx.x >> 6);
  const unsigned bid = blockIdx.x;
  {
    u64 pc;
    asm volatile("s_getpc_b64 %0" : "=s"(pc));
    dma4(mk((const void*)pc, CODE_BYTES), oob_unless((threadIdx.x & 63) < 6, threadIdx.x * 12 + (threadIdx.x & 63) * 116), (bid >> 3) * 3072, L_KVF);
    SB();
  }
#define ROLES(B) \
  const unsigned xcd = (B) & 7, r = (B) >> 3, odd = xcd & 1; \
  unsigned j = 4 * (xcd >> 1) + (r >> 3), h = 8 * odd + (r & 7); \
  const unsigned r7 = (r * 37) >> 8, r3 = (r * 43) >> 7;     \
  const bool act2 = r < 28, act1 = r < 28 - 7 * odd; \
  unsigned t = 4 * xcd + (r7 & 3), ds = r - 7 * r7; \
  unsigned ds1 = odd * (4 + r - 3 * r3) + (1 - odd) * (r & 3); \
  unsigned fs = umin_(27u, 7 * (xcd >> 1) + odd * r3 + (1 - odd) * (r >> 2));
  ROLES(bid)
  v4u raw[4];
  unsigned vE;
  auto xrow_off = [&](unsigned ln, int half) INL { const unsigned i = (ln >> 3) + 8 * half; return i * 896 + (((ln & 7) ^ (i >> 1)) * 16); };
  const unsigned ch = (lane & 3) ^ (2 * ((lane >> 4) & 1));
  {
    const unsigned j7 = (j * 37) >> 8, T_ = 4 * t + w;
    unsigned wqv = (lane >> 2) * 2688 + (ch == 3 ? 40 : ch * 16), xv = xrow_off(lane, 0);
    unsigned s0 = (112 * w + 16 * (j - 7 * j7)) * 2688 + (j7 == 2 ? 0 : j7 + 1) * 896 + h * 56, s1 = (112 * w + 16 * (j + 2)) * 2688 + h * 56;
    unsigned sx = 16 * T_ * 896 + ds * 128;
    OPV(wqv); OPV(xv); OPQ(s0); OPQ(s1); OPQ(sx);
    SB();
    ld16h(raw[0], mk(gwqkv, 448 * 1344 * 2), wqv, s0);
    ld16h(raw[2], mk(gx, act2 ? 2048 * 448 * 2 : 0), xv, sx);
    ld16h(raw[3], mk(gx, act2 ? 2048 * 448 * 2 : 0), (xv ^ 64) + 8 * 896, sx);
    ld16h(raw[1], mk(gwqkv, j < 5 ? 448 * 1344 * 2 : 0), wqv, s1);
    asm volatile("s_barrier" ::: "memory");
    asm volatile("buffer_load_dword %0, off, %1, 0 sc1" : "=v"(vE) : "s"(mk(gws, 64)) : "memory");
    SB();
  }
  const unsigned gw = bid * 4 + w;
  const unsigned n1 = act1 ? WS_BYTES : 0, n2 = act2 ? WS_BYTES : 0;
  const rsrc_t rW = mk(gws, WS_BYTES), r1 = mk(gws, n1), r2 = mk(gws, n2);
  unsigned l16 = lane * 16;
  OPV(l16);
  const v4u ONES = {0x3f803f80u, 0x3f803f80u, 0x3f803f80u, 0x3f803f80u};
  const f32x4 Z4 = {0.f, 0.f, 0.f, 0.f};
  STAMP(0);
#define T (4 * t + w)
#define cA ((j * 16 + h) * 4 + w)
#define c1 ((ds1 * 28 + fs) * 4 + w)
#define c2 ((t * 7 + ds) * 4 + w)
#define oKV (OFF_KVP + ((h * 4 + w) * 16 + j) * 512)
#define oO (OFF_O + ((4 * j + w) * 16 + h) * 2048)
#define oH (OFF_H + ((ds * 64 + 2 * t + (w >> 1)) * 4 + 2 * (w & 1)) * 1024)
#define oU (OFF_U + ((ds1 * 28 + fs) * 8 + w) * 1024)
#define oS (OFF_STAT + T * 8 * 128)
#define oX (OFF_X2 + (T * 14 + 2 * ds) * 1024)
  unsigned w1s = 512 * w * 3584 + fs * 128, w2s = 448 * w * 4096 + t * 128;
#define CODE_AHEAD() do { u64 pc_; asm volatile("s_getpc_b64 %0" : "=s"(pc_)); \
    asm volatile("s_mov_b32 m0, %2\n\ts_mov_b64 exec, 1\n\tbuffer_load_dword off, %0, %1 lds\n\ts_mov_b64 exec, -1" \
                 ::"s"(mk((const void*)pc_, 16384)), "s"(cw128), "s"(L_CS + 384) : "memory"); } while (0)
  const unsigned cw128 = (r * 4 + w) * 128;
#define FRESH() do { OPQ(w); OPQ(j); OPQ(h); OPQ(t); OPQ(ds); OPQ(ds1); OPQ(fs); OPQ(w1s); OPQ(w2s); \
    asm volatile("v_mov_b32 %0, %3\n\tv_mov_b32 %1, %4\n\tv_mov_b32 %2, %5" \
                 : "=&v"(l16), "=&v"(vE), "=&v"(wv1) : "v"(l16), "s"((unsigned)EE), "v"(wv1)); } while (0)

  const rsrc_t rO = mk(gwo, 448 * 448 * 2);
  v4u xf[2][14], W[64], W2[56];
  const rsrc_t rA = mk(gw1, act1 ? 2048 * 1792 * 2 : 0), rB = mk(gw2, 2048 * 1792 * 2);
  v2u xr[4];
  f32x4 ak[2][2], av[2][2], ss[2];
  unsigned budget = SPIN_CAP, wq0, wq1;
  unsigned wv1, wv2, cvr[4], w4k = w * 4096;
  {
    const unsigned x = lane >> 3, c = ((lane & 7) ^ (2 * ((x >> 1) & 3))) * 16;
    wv1 = x * 3584 + c;
    OPV(wv1); OPQ(w4k);
  }
  auto cv_prep = [&](int second) INL {
    LANE_COPY();
    cvr[0] = (w4k + (ln >> 2) * 128 + ((ln >> 3) & 3) * 32 + (ln & 3) * 8) ^ ((second ? w : 2 * (w & 1)) * 32);
    OPV(cvr[0]);
#pragma unroll
    for (int a = 1; a < 4; ++a) { cvr[a] = cvr[0] ^ (a * 32); OPV(cvr[a]); }
  };
  u64 EE;
#define ALLONES() unsigned n1v = ~0u; OPV(n1v); const v4u N4 = {n1v, n1v, n1v, n1v}
  unsigned e_bp, e_cs, e_and, e_or, e_kw, e_kr, e_fa, e_st, a_st, b_st, c_st;
  auto epi_prep = [&](int L) INL {
    LANE_COPY();
    e_bp = 80 * g; e_cs = L_CS + li * 4;
    e_and = (li >> 2) == 2 ? 0u : ~0u; e_or = ~e_and & 0x3f803f80u;
    e_kw = L_KVR + w * 1024 + l16; e_kr = L_KVR + w * 4096 + l16;
    e_fa = oob_unless(ln < 16, ln * (64 * MBS) + (OFF_MB + L * MB_L + HA * MB_HOP + (h * 4 + w) * MBS + sl16(j)));
    e_st = ln * 8 + (oKV + L * KVP_L);
    OPV(e_bp); OPV(e_cs); OPV(e_and); OPV(e_or); OPV(e_kw); OPV(e_kr); OPV(e_fa); OPV(e_st);
  };
  auto pub_prep = [&](int L) INL {
    LANE_COPY();
    a_st = l16 + (oO + L * O_L);
    b_st = oob_unless(ln < 16, ln * 8 + (oS + L * STAT_L + ds * 128));
    c_st = l16 + (oH + L * H_L);
    OPV(a_st); OPV(b_st); OPV(c_st);
  };
  auto wq_base = [&]() INL {
    LANE_COPY();
    const unsigned pp = ln & 3;
    wq0 = L_WQ + (4 * g + ((ln >> 2) & 3)) * 64 + (((pp >> 1) ^ (2 * (g & 1))) * 16) + (pp & 1) * 8;
    wq1 = wq0 ^ 32;
    OPV(wq0); OPV(wq1);
  };
  auto w_dma = [&](auto lc, auto kc) INL {
    constexpr int L = lc.v, k = kc.v, u = k >> 2;
    constexpr unsigned dst = slot_at(WP[L].slot[u]) + (k & 3) * 1024;
    if constexpr (k == 4 * nu1(L)) { wv2 = wv1 + (l16 & 0x380) * 4; OPV(wv2); }
    constexpr bool one = u < nu1(L);
    constexpr unsigned src = one ? (4 * w1k(L, u) + (k & 3)) * (8 * 3584) : (k - 4 * nu1(L)) * (8 * 4096);
    const unsigned base = w4k, sb = one ? w1s : w2s, v = one ? wv1 : wv2;
    const rsrc_t rs = one ? rA : rB;
    unsigned so;
    asm volatile("s_add_u32 m0, %1, %2\n\ts_add_u32 %0, %3, %4\n\tbuffer_load_dwordx4 %5, %6, %0 offen lds ; WD %7 %8"
                 : "=&s"(so) : "s"(base), "n"(dst), "s"(sb), "n"(src), "v"(v), "s"(rs), "n"(L * 100 + u), "n"(WP[L].slot[u]) : "memory", "scc");
  };
  auto w_rd = [&](auto lc, auto uc, auto ac) INL {
    constexpr int L = lc.v, u = uc.v, a = ac.v;
    constexpr unsigned at = slot_at(WP[L].slot[u]);
    if constexpr (a == 0 && (u == 0 || u == u0(L) || u == nu1(L))) cv_prep(u == nu1(L));
    asm volatile("; WR %0 %1" ::"n"(L * 100 + u), "n"(WP[L].slot[u]) : "memory");
    unsigned b = cvr[a];
    if constexpr (at >= L_Z) { b += L_Z; OPV(b); }
    constexpr unsigned o = at >= L_Z ? at - L_Z : at;
    if constexpr (L == 0 && u < NR) {
      constexpr int z = RA0 + 16 * u + 4 * a;
      asm volatile("ds_read_b64_tr_b16 a[%c1:%c2], %0 offset:%5\n\tds_read_b64_tr_b16 a[%c3:%c4], %0 offset:%6"
                   ::"v"(b), "n"(z), "n"(z + 1), "n"(z + 2), "n"(z + 3), "n"(o), "n"(o + 2048) : "memory");
    } else if constexpr (u < nu1(L)) W[4 * w1k(L, u) + a] = trf(b + o, b + o + 2048);
    else W2[4 * (u - nu1(L)) + a] = trf(b + o, b + o + 2048);
    asm volatile("; WE" ::: "memory");
  };
  auto img_dma = [&](int u) INL { dma16c(rW, l16, OFF_WQI + ((h * 4 + w) * 21 + u) * 1024, L_WQ + (u / 7) * 28672 + (7 * w + u % 7) * 1024); };
  auto w_touch = [&](int x, int y) INL {
    const unsigned v = cw128 >> 7, ln = l16 >> 4;
    if (x == 0) {
      const unsigned i = (odd * 128 + v) * 4, l7 = (ln * 37) >> 8;
      dma4(mk(gw1, 2048 * 1792 * 2), oob_unless(ln < 28, l7 * 3584 + (ln - 7 * l7) * 128), ((i >> 8) * 512 + ((32 * RK0 + (1 - y) * 256 + (i & 255)) & 511)) * 3584 + (xcd >> 1) * 896, L_JUNK);
    } else
      dma4(rB, oob_unless(ln < 28, (ln >> 2) * 4096 + (ln & 3) * 128), ((v >> 5) * 448 + y * 224 + (v & 31) * 7) * 4096 + xcd * 512, L_JUNK);
  };
  auto wo_dma = [&](unsigned v, int u) INL { dma16(rO, v, (112 * w + 8 * u) * 896 + ds * 128, L_R + (14 * w + u) * 1024); };
  auto wo_voff = [&]() INL { LANE_COPY(); const unsigned x = ln >> 3; return x * 896 + (((ln & 7) ^ (2 * ((x >> 1) & 3))) * 16); };
  v4u wf[14][5];
  unsigned xo, l16x;
  auto wf_ld = [&](int i, int f) INL {
    const unsigned b = (f == 4 ? (wq0 ^ ((w & 1) * 32)) + (w >> 1) * 28672 : ((f & 1) ? wq1 : wq0) + (f >> 1) * 28672) + pm(i) * 2048;
    if (i < 14) wf[i][f] = trf(b, b + 1024);
  };
  auto x_prep = [&](int L) INL {
    wq_base();
    xo = OFF_X2 + (1 - L) * X_L + (8 * j + 2 * w) * 14 * 1024; l16x = l16 + 14336;
    OPQ(xo); OPV(l16x);
  };
  auto x_ld = [&](int n) INL { const int m = pm(n >> 1); xf[n & 1][n >> 1] = ld16a(rW, ((n & 1) ? l16x : l16) + (m & 3) * 1024, xo + (m >> 2) * 4096); };
  auto x_ldh = [&](auto nc) INL { constexpr int n = nc.v, m = pm(n >> 1); ld16hc<(m & 3) * 1024>(xf[n & 1][n >> 1], rW, (n & 1) ? l16x : l16, xo + (m >> 2) * 4096); };
  {
    const unsigned li = lane & 15, g = lane >> 4;
    const rsrc_t rI2 = mk(gws, j < 5 ? WS_BYTES : 0);
    {
      const unsigned hop = (lane >> 1) & 7, id = hop == HD ? c1 : (hop == HB || hop == HC || hop == HE) ? c2 : hop == HW2 ? gw : cA;
      dma4(rW, oob_unless(lane < 32 && ((0x57558f55u >> lane) & 1), (lane >> 1) * MB_HOP + id * MBS + (lane & 1) * 128), OFF_MB, L_KVF);
    }
    dma4(rO, oob_unless(lane < 4, lane * 128), gw * 512, L_JUNK);
    if (TAP == 0) w_touch(0, 1);
    dma4(mk(gb1, 1792 * 2), lane * 4, (fs >> 1) * 256, L_B1);
    dma4(mk(gb2, 2048 * 2), lane * 4, (t >> 1) * 256, L_B2);
    unsigned vi = l16 + OFF_WQI + ((h * 4 + w) * 21 + j) * 1024, vx = l16 + oX + X_L;
    OPV(vi); OPV(vx);
    unsigned xa[4], fi, fx;
#pragma unroll
    for (int c = 0; c < 4; ++c) { xa[c] = L_XS + w * 4096 + (li >> 3) * 1024 + (li & 7) * 128 + (g & 1) * 8 + (((2 * c + (g >> 1)) ^ (li >> 1)) * 16); OPV(xa[c]); }
    fi = oob_unless(lane < 16, lane * (64 * MBS) + (OFF_MB + MB_L + HW1 * MB_HOP + (h * 4 + w) * MBS + sl16(j)));
    fx = oob_unless(lane < 16, lane * (4 * MBS) + (OFF_MB + MB_L + HF * MB_HOP + ((T >> 3) * 64 + ((T & 7) >> 1)) * MBS + (T & 1) * 64 + ds * 4));
    OPV(fi); OPV(fx);
    const bool ones = j < 7 && ch == 3;
    FENCE();
    ldw<6 - CODE1 + (TAP == 0)>(raw[0], raw[1], raw[2], raw[3]);
    STAMP(1);
    lds_st<v4u>(L_XS + w * 4096 + l16, raw[2]);
    lds_st<v4u>(L_XS + w * 4096 + 1024 + l16, raw[3]);
#pragma unroll
    for (int c = 0; c < 4; ++c) xr[c] = lds_ld<v2u>(xa[c]);
    raw[0].x = ones ? 0x3f803f80u : raw[0].x;
    raw[0].y = ones ? 0x3f803f80u : raw[0].y;
    st16c(raw[0], rW, vi, 0);
#pragma unroll
    for (int e = 0; e < 2; ++e) st16c(cat(xr[2 * e], xr[2 * e + 1]), r2, vx, e * 1024);
    st16c(raw[1], rI2, vi, 16384);
    FENCE();
    if (TAP == 1) { w_touch(0, 1); VMW(1); } else VMW(0);
    OPV(vE);
    vE += 1;
    st4c(vE, rW, fi, 0);
    st4c(vE, r2, fx, 0);
    FENCE();
    { const unsigned E = __builtin_amdgcn_readfirstlane(vE); EE = (u64)E << 32 | E; }
  }
  FRESH();

  unsigned d_st, f_st;
    v4u wof[16][4];
    unsigned wa[2][4];
    auto wo_prep = [&]() INL {
      LANE_COPY();
#pragma unroll
      for (int jh = 0; jh < 2; ++jh) {
        const unsigned row = (jh ? 16 + 4 * ((g + 1) >> 1) : 4 * g) + ((ln >> 2) & 3);
        wa[jh][0] = L_R + row * 128 + (((row >> 1) & 3) * 32) + (ln & 3) * 8;
        OPV(wa[jh][0]);
#pragma unroll
        for (int c = 1; c < 4; ++c) { wa[jh][c] = wa[jh][0] ^ (c * 32); OPV(wa[jh][c]); }
      }
    };
    auto wof_ld = [&](int hh, int c) INL { const int cc = (hh & 1) ? c ^ 2 : c; wof[hh][c] = trf(wa[0][cc] + hh * 3584, wa[1][cc] + hh * 3584); };
#define LAYER_DEFS \
    constexpr int L = lc.v; \
    constexpr unsigned mb = OFF_MB + L * MB_L; \
    constexpr int NW1 = L ? 64 - NRES : 64; \
    auto wd = [&](auto kc) INL { if constexpr (!IDLE) w_dma(IC<L>{}, kc); }; \
    auto wdu = [&](auto uc, auto pc) INL { wd(IC<4 * (u0(L) + uc.v) + pc.v>{}); }; \
    auto wr = [&](auto uc, auto ac) INL { if constexpr (!IDLE) w_rd(IC<L>{}, uc, ac); }; \
    auto wru = [&](auto uc) INL { static_for<0, 4>([&](auto ac) INL { wr(IC<u0(L) + uc.v>{}, ac); }); }; \
    constexpr int WOP = L ? WOP1 : WOP0;

  auto front = [&](auto lc, auto idle) INL {
    constexpr bool IDLE = idle.v;
    LAYER_DEFS
    unsigned padm, a_kw, a_z, a_fb, q_src;
    v4u SEL;
    auto a_prep = [&]() INL {
      LANE_COPY();
      padm = g == 2 ? 0u : ~0u;
      a_kw = L_KVF + (w & 1) * 1024 + (w >> 1) * 8 + li * 32 + (g >> 1) * 512 + (g & 1) * 16; a_z = L_KVF + 1024 + (16 * g + 8) * 16;
      a_fb = oob_unless(ln < 14, ((ln >> 1) * 4 + (ln & 1)) * MBS + (mb + HB * MB_HOP + ((2 * j + (w >> 1)) * 28 + 2 * (w & 1)) * MBS + sl16(h)));
      const unsigned one = (g & 1) == (li >> 3) ? 0x3f80u << (16 * (li & 1)) : 0u, d = (li >> 1) & 3;
      SEL = v4u{d == 0 ? one : 0u, d == 1 ? one : 0u, d == 2 ? one : 0u, d == 3 ? one : 0u};
      OPV(padm); OPV(a_kw); OPV(a_z); OPV(a_fb); OPV(SEL);
    };
    f32x4 acs, aq[2][2];
    v4u qf[14][2];
    unsigned qb0, qb1;
#define QF(m) qf[m][0] = trf(qb0 + pm(m) * 2048, qb0 + pm(m) * 2048 + 1024), qf[m][1] = trf(qb1 + pm(m) * 2048, qb1 + pm(m) * 2048 + 1024)
    auto q_ld = [&](int m, int y) INL { const unsigned b = (y ? qb1 : qb0) + pm(m) * 2048; return trf(b, b + 1024); };
    {
      const unsigned mbF = OFF_MB + (1 - L) * MB_L + HF * MB_HOP + cA * MBS;
      v16u pkF;
      unsigned wv;
      if constexpr (L == 0) {
        x_prep(0);
        const unsigned mbI = OFF_MB + MB_L + HW1 * MB_HOP + cA * MBS;
        static_for<0, 12>([&](auto kc) INL { w_dma(IC<0>{}, kc); });
        wv = wo_voff();
        epi_prep(0);
        pub_prep(0);
        a_prep();
        OPV(wv);
        pkF = peek<16>(gws, mbI);
        FENCE();
        poll<16>(pkF, gws, mbI, EE, 1, budget);
        pkF = peek<14>(gws, mbF);
#pragma unroll
        for (int u = 0; u < 14; ++u) img_dma(u);
        FENCE();
        poll<14>(pkF, gws, mbF, EE, 1, budget);
        STAMP(19);
        VMW(14);
        static_for<0, XI0>([&](auto ic) INL { x_ldh(ic); w_rd(IC<0>{}, IC<ic.v / 4>{}, IC<ic.v % 4>{}); SB(); });
        asm volatile("s_waitcnt vmcnt(%0)" ::"n"(XI0) : "memory");
        BAR();
#pragma unroll
        for (int x = 0; x < 10; ++x) wf_ld(x / 5, x % 5);
        SB();
      } else {
        asm volatile("s_barrier" ::: "memory");
        STAMP(20);
      }
      constexpr int PM = L ? 10 : 11;
      static_for<0, 14 * PM>([&](auto ic) INL {
        constexpr int n = ic.v, m = n / PM, q = n % PM, tl = q / 5, f = q % 5;
        if constexpr (q < 10 && f == 0 && (L == 0 || m >= 7)) ldw1<p1_younger(L, IDLE, n, 2 * m + tl)>(xf[tl][m]);
        if constexpr (q == 10) acs = mfma(ONES, wf[m][4], m ? acs : Z4);
        else if constexpr (f == 0) ss[tl] = mfma(xf[tl][m], xf[tl][m], m ? ss[tl] : Z4);
        else if constexpr (f < 3) ak[tl][2 - f] = mfma(xf[tl][m], wf[m][2 - f], m ? ak[tl][2 - f] : Z4);
        else av[tl][f - 3] = mfma(xf[tl][m], wf[m][f - 1], m ? av[tl][f - 3] : Z4);
        SB();
        if constexpr (L == 0) {
          if constexpr (n % 4 == 0) {
            constexpr int u = XI0 + n / 4;
            if constexpr (u == TB && (TW & 2)) w_touch(0, 0);
            if constexpr (u == 50 && TCP == 1) w_touch(1, 0);
            if constexpr (u < 28) x_ldh(IC<u>{});
            else if constexpr (u < 35) img_dma(14 + u - 28);
            else if constexpr (u < 49 - WO0) wo_dma(wv, WO0 + u - 35);
            else if constexpr (u == 49 - WO0) {
              ALLONES();
              st8c(v2u{n1v, n1v}, rW, e_st, 0);
              FENCE();
              st16c(N4, rW, a_st, 0); st16c(N4, rW, a_st, 1024); st8c(v2u{n1v, n1v}, r2, b_st, 0);
              st16c(N4, r2, c_st, 0); st16c(N4, r2, c_st, 1024);
            }
            static_assert(XI0 + 38 >= 49 - WO0, "");
          }
        } else {
          constexpr int X0 = (69 - PLF1) / 4 + 1;
          if constexpr (n == 3) { wv = wo_voff(); OPV(wv); }
          if constexpr (n == WOS) {
#pragma unroll
            for (int y = 0; y < 14; ++y) OPV(xf[y & 1][y >> 1]);
          }
          if constexpr (!IDLE && p1_wo(n) >= 0) wo_dma(wv, p1_wo(n));
          if constexpr (n == PKF1) pkF = peek<14>(gws, mbF + 128);
          if constexpr (n == PLF1) poll<14>(pkF, gws, mbF + 128, EE, 1, budget);
          if constexpr (n >= PLF1 && n <= 69 && (n - PLF1) % 4 == 0) x_ldh(IC<14 + (n - PLF1) / 4>{});
          if constexpr (n == 45) { qb0 = wq0 + 57344; qb1 = qb0 ^ 32; OPV(qb0); OPV(qb1); }
          if constexpr (n >= 47 && n < 70 && n % 4 == 3 && (n - 47) / 4 < 6) qf[(n - 47) / 8][((n - 47) / 4) & 1] = q_ld((n - 47) / 8, ((n - 47) / 4) & 1);
          if constexpr (n == 69) {
            static_for<0, 7>([&](auto qc) INL {
              constexpr int y = qc.v;
              aq[0][0] = mfma(qf[y][0], xf[0][y], y ? aq[0][0] : Z4);
              SB();
              if constexpr (y + 3 < 7) qf[y + 3][0] = q_ld(y + 3, 0);
              SB();
              aq[1][0] = mfma(qf[y][0], xf[1][y], y ? aq[1][0] : Z4);
              SB();
              if constexpr (y + 3 < 7) qf[y + 3][1] = q_ld(y + 3, 1);
              SB();
              aq[0][1] = mfma(qf[y][1], xf[0][y], y ? aq[0][1] : Z4);
              SB();
              x_ldh(IC<14 + X0 + y>{});
              SB();
              aq[1][1] = mfma(qf[y][1], xf[1][y], y ? aq[1][1] : Z4);
              SB();
            });
            epi_prep(1);
          }
          if constexpr (n > 69 && n % 4 == 0 && X0 + 7 + (n - 72) / 4 < 14) x_ldh(IC<14 + X0 + 7 + (n - 72) / 4>{});
          if constexpr (n % 4 == 2) wf_ld(WFP + n / 16, (n / 4) & 3);
        }
        if constexpr (L == 0 && q % 2 == 1) wf_ld(m + 2, q / 2);
        SB();
      });
      if constexpr (L == 0) lds_st<float>(e_cs + w * 64, acs[0] * (-1.f / 448.f));
      if (L == 1 && UNLIKELY(anynan(ss[0][0], ss[1][0]))) {
        REDO_COUNT(4);
#pragma unroll
        for (int y = 0; y < 4; ++y) {
          v4u z[7];
          ld_settled<7>(z, rW, l16, xo + (y & 1) * 14336 + (y >> 1) * 1024, 2048);
#pragma unroll
          for (int i = 0; i < 7; ++i) xf[y & 1][7 * (y >> 1) + i] = z[i];
        }
#pragma unroll
        for (int m = 0; m < 14; ++m) {
#pragma unroll
          for (int f = 0; f < 4; ++f) wf_ld(m, f);
#pragma unroll
          for (int tl = 0; tl < 2; ++tl) {
            ss[tl] = mfma(xf[tl][m], xf[tl][m], m ? ss[tl] : Z4);
            ak[tl][1] = mfma(xf[tl][m], wf[m][1], m ? ak[tl][1] : Z4);
            ak[tl][0] = mfma(xf[tl][m], wf[m][0], m ? ak[tl][0] : Z4);
            av[tl][0] = mfma(xf[tl][m], wf[m][2], m ? av[tl][0] : Z4);
            av[tl][1] = mfma(xf[tl][m], wf[m][3], m ? av[tl][1] : Z4);
          }
          if (m < 7) {
            QF(m);
#pragma unroll
            for (int y = 0; y < 4; ++y) aq[y & 1][y >> 1] = mfma(qf[m][y >> 1], xf[y & 1][m], m ? aq[y & 1][y >> 1] : Z4);
          }
          SB();
        }
      }
      if constexpr (L == 1) STAMP(21);
    }
    f32x2 rs2[4], sm2[4];
    {
      auto hook = [&](auto hc) INL {
        SB();
        if constexpr (hc.v >= 2 && hc.v < 14) wd(IC<4 * u0(L) + hc.v - 2>{});
        KEEP();
      };
      f32x2 sq2[4];
      OPV(ss[0]); OPV(ss[1]);
#pragma unroll
      for (int x = 0; x < 4; ++x) sq2[x] = f32x2{bperm(e_bp + 8 * (x & 1), ss[x >> 1][2 * (x & 1)]), bperm(e_bp + 8 * (x & 1) + 4, ss[x >> 1][2 * (x & 1) + 1])};
      hook(IC<0>{});
      OPV(ak[0][1]); OPV(ak[1][1]);
#pragma unroll
      for (int x = 0; x < 4; ++x)
        sm2[x] = f32x2{asf(__builtin_amdgcn_mov_dpp((int)asu(ak[x >> 1][1][2 * (x & 1)]), 0x158, 0xf, 0xf, true)),
                       asf(__builtin_amdgcn_mov_dpp((int)asu(ak[x >> 1][1][2 * (x & 1) + 1]), 0x158, 0xf, 0xf, true))};
      hook(IC<1>{});
      BAR();
      STAMP(2 + L * 20);
      float cs[4];
#pragma unroll
      for (int n = 0; n < 4; ++n) cs[n] = lds_ld<float>(e_cs + n * 64);
      f32x2 mu[4];
#pragma unroll
      for (int x = 0; x < 4; ++x) { mu[x] = sm2[x] * bc(1.f / 448.f); sq2[x] = fma2(sq2[x], bc(1.f / 448.f), bc(1e-5f)); }
#pragma unroll
      for (int x = 0; x < 4; ++x) sq2[x] = fma2(-mu[x], mu[x], sq2[x]);
      hook(IC<2>{});
#pragma unroll
      for (int x = 0; x < 4; ++x) sq2[x] = f32x2{fmaxf(sq2[x].x, 1e-5f), fmaxf(sq2[x].y, 1e-5f)};
#pragma unroll
      for (int x = 0; x < 2; ++x) rs2[x] = f32x2{__builtin_amdgcn_rsqf(sq2[x].x), __builtin_amdgcn_rsqf(sq2[x].y)};
      hook(IC<3>{});
#pragma unroll
      for (int x = 2; x < 4; ++x) rs2[x] = f32x2{__builtin_amdgcn_rsqf(sq2[x].x), __builtin_amdgcn_rsqf(sq2[x].y)};
      f32x2 ka[8], ke[8];
      unsigned kp[8], vp[8];
#pragma unroll
      for (int y = 4; y < 8; ++y) ka[y] = fma2(sm2[y & 3], bc(cs[1]), half2(ak[(y >> 1) & 1][1], y & 1));
      hook(IC<4>{});
      OPV(ak[0][0]); OPV(ak[1][0]);
#pragma unroll
      for (int y = 0; y < 4; ++y) ka[y] = fma2(sm2[y], bc(cs[0]), half2(ak[y >> 1][0], y & 1));
#pragma unroll
      for (int x = 0; x < 4; ++x) mu[x] = rs2[x] * bc(LOG2E);
      hook(IC<5>{});
#pragma unroll
      for (int y = 0; y < 8; ++y) ke[y] = ka[y] * mu[y & 3];
#pragma unroll
      for (int y = 0; y < 8; ++y) ka[y] = fma2(ka[y], rs2[y & 3], bc(1.f));
      hook(IC<6>{});
#pragma unroll
      for (int y = 0; y < 8; ++y) {
        ke[y] = f32x2{__builtin_amdgcn_exp2f(ke[y].x), __builtin_amdgcn_exp2f(ke[y].y)};
        if (y == 3) hook(IC<7>{});
        if (y == 7) hook(IC<8>{});
      }
#pragma unroll
      for (int y = 0; y < 8; ++y) ke[y] = f32x2{__builtin_amdgcn_fmed3f(ke[y].x, ka[y].x, 1.f), __builtin_amdgcn_fmed3f(ke[y].y, ka[y].y, 1.f)};
      hook(IC<9>{});
#pragma unroll
      for (int y = 0; y < 8; ++y) ke[y] += bc(1e-6f);
#pragma unroll
      for (int y = 0; y < 8; ++y) kp[y] = pk2(ke[y]);
      hook(IC<10>{});
      if constexpr (FA_AT == 1) { asm volatile("s_waitcnt vmcnt(%0)" ::"n"(IDLE ? 0 : L ? 9 : 14) : "memory"); st4c(vE, rW, e_fa, 0); }
#pragma unroll
      for (int y = 0; y < 8; ++y) {
        if ((y & 1) == 0) OPV(av[(y >> 1) & 1][y >> 2]);
        ka[y] = fma2(sm2[y & 3], bc(cs[2 + (y >> 2)]), half2(av[(y >> 1) & 1][y >> 2], y & 1));
        if (y == 3) hook(IC<11>{});
        if (y == 7) hook(IC<12>{});
      }
#pragma unroll
      for (int y = 0; y < 8; ++y) ka[y] *= rs2[y & 3];
#pragma unroll
      for (int y = 0; y < 8; ++y) vp[y] = pk2(ka[y]);
      hook(IC<13>{});
#pragma unroll
      for (int y = 4; y < 8; ++y) vp[y] = (vp[y] & e_and) | e_or;
      f32x4 kv[4];
#pragma unroll
      for (int x = 0; x < 4; ++x) {
        const int a = x & 1, b = x >> 1;
        kv[x] = mfma(v4u{kp[4 * a], kp[4 * a + 1], kp[4 * a + 2], kp[4 * a + 3]}, v4u{vp[4 * b], vp[4 * b + 1], vp[4 * b + 2], vp[4 * b + 3]}, Z4);
      }
      if constexpr (FA_AT == 2) { asm volatile("s_waitcnt vmcnt(%0)" ::"n"(IDLE ? 0 : L ? 12 : 17) : "memory"); st4c(vE, rW, e_fa, 0); }
      hook(IC<14>{});
      hook(IC<15>{});
#pragma unroll
      for (int x = 0; x < 4; ++x) lds_st<f32x4>(e_kw + (2 * (x & 1) + (x >> 1)) * 4096, kv[x]);
      BAR();
#pragma unroll
      for (int x = 0; x < 4; ++x) kv[x] = lds_ld<f32x4>(e_kr + x * 1024);
      SB();
      { const f32x4 z = (kv[0] + kv[1]) + (kv[2] + kv[3]); st8c(v2u{pk2(lo2(z)), pk2(hi2(z))}, rW, e_st, 0); }
      FENCE();
    }
    STAMP(3 + L * 20);
    FRESH();
    if constexpr (!IDLE) CODE_AHEAD();
    const unsigned mbA = mb + HA * MB_HOP + cA * MBS;
    unsigned so_a = OFF_KVP + L * KVP_L + (h * 4 + w) * 8192;
    OPQ(so_a);
    v16u pkA;
    f32x4 csq[2];
    float rq[2], sq_[2];
    unsigned qp[8];
    f32x2 qa[8], qe[8];
    float rl[2];
    auto q_epi = [&](auto ic) INL {
      constexpr int i = ic.v;
      if constexpr (i < 2) {
        rl[i] = rq[i] * LOG2E;
        OPV(aq[i][0]); OPV(aq[i][1]);
#pragma unroll
        for (int y = 4 * i; y < 4 * i + 4; ++y) qa[y] = fma2(bc(sq_[i]), half2(csq[(y >> 1) & 1], y & 1), half2(aq[i][(y >> 1) & 1], y & 1));
      } else if constexpr (i == 2) {
#pragma unroll
        for (int y = 0; y < 8; ++y) qe[y] = qa[y] * bc(rl[y >> 2]);
#pragma unroll
        for (int y = 0; y < 4; ++y) qa[y] = fma2(qa[y], bc(rq[0]), bc(1.f));
      } else if constexpr (i < 6) {
#pragma unroll
        for (int y = 3 * (i - 3); y < 3 * (i - 3) + 3 && y < 8; ++y) qe[y] = f32x2{__builtin_amdgcn_exp2f(qe[y].x), __builtin_amdgcn_exp2f(qe[y].y)};
        if constexpr (i == 5) {
#pragma unroll
          for (int y = 4; y < 8; ++y) qa[y] = fma2(qa[y], bc(rq[1]), bc(1.f));
        }
      } else if constexpr (i < 8) {
#pragma unroll
        for (int y = 4 * (i - 6); y < 4 * (i - 6) + 4; ++y) {
          qe[y] = f32x2{__builtin_amdgcn_fmed3f(qe[y].x, qa[y].x, 1.f), __builtin_amdgcn_fmed3f(qe[y].y, qa[y].y, 1.f)} + bc(1e-6f);
          qp[y] = pk2(qe[y]);
          if (y & 2) qp[y] &= padm;
          OPV(qp[y]);
        }
      }
    };
    constexpr int Q0 = L ? 7 : 0, Q1 = L ? 14 : QS0;
    float t0[4];
    v4u NM;
    unsigned ca, li3;
    auto q_step = [&](auto ic, auto&& req) INL {
      constexpr int m = ic.v, z = m - Q0;
      aq[0][0] = mfma(qf[m][0], xf[0][m], m ? aq[0][0] : Z4);
      SB();
      req();
      SB();
      aq[1][0] = mfma(qf[m][0], xf[1][m], m ? aq[1][0] : Z4);
      SB();
      if constexpr (m + 3 < 14) qf[m + 3][0] = q_ld(m + 3, 0);
      if constexpr (m == 12) { rq[0] = bperm(q_src, t0[0]); sq_[0] = bperm(q_src, t0[1]); }
      if constexpr (m == 13) { rq[1] = bperm(q_src, t0[2]); sq_[1] = bperm(q_src, t0[3]); }
      SB();
      aq[0][1] = mfma(qf[m][1], xf[0][m], m ? aq[0][1] : Z4);
      SB();
      if constexpr (m + 3 < 14) qf[m + 3][1] = q_ld(m + 3, 1);
      if constexpr (m == 12 && L == 1) { csq[0] = lds_ld<f32x4>(ca); csq[1] = lds_ld<f32x4>(ca + 64); }
      SB();
      aq[1][1] = mfma(qf[m][1], xf[1][m], m ? aq[1][1] : Z4);
      SB();
      if constexpr (z >= 1 && z < 5) {
        constexpr int x = z - 1;
        const f32x2 a0 = (x & 1) ? sm2[x & 2] : rs2[x & 2], a1 = (x & 1) ? sm2[(x & 2) + 1] : rs2[(x & 2) + 1];
        float u0_ = sel(li3 & 16, a0.y, a0.x), u1_ = sel(li3 & 16, a1.y, a1.x);
        OPV(u0_); OPV(u1_);
        t0[x] = sel(li3 & 32, u1_, u0_);
        OPV(t0[x]);
      }
      if constexpr (m == (L ? PKA1 + 7 : PKA0)) pkA = peek<16>(gws, mbA);
      if constexpr (L == 0) {
        SB();
        csq[0] = mfma(qf[m][0], ONES, m ? csq[0] : Z4);
        csq[1] = mfma(qf[m][1], ONES, m ? csq[1] : Z4);
      }
      SB();
    };
    if constexpr (L == 0) { qb0 = wq0 + 57344; qb1 = qb0 ^ 32; OPV(qb0); OPV(qb1); }
    QF(Q0); QF(Q0 + 1); QF(Q0 + 2);
    li3 = l16; ca = L_CSQ + ((l16 >> 4) & 48); q_src = (l16 & 0xc0) + ((l16 >> 2) & 60);
    OPV(ca); OPV(q_src);
    SB();
    static_for<Q0, Q1>([&](auto ic) INL { q_step(ic, [&]() INL { if constexpr (ic.v - Q0 < 4) wdu(IC<3>{}, IC<ic.v - Q0>{}); }); });
    if constexpr (L == 1) { pub_prep(1); a_prep(); }
    STAMP(4 + L * 20);
    {
      const unsigned so = so_a;
      poll<16>(pkA, gws, mbA, EE, 1, budget);
      STAMP(5 + L * 20);
      v4u p[8];
      f32x4 ps;
      constexpr int NQ = 14 - Q1, NT = L == 0 && TCP == 0, NBH = IDLE ? 0 : 8 + NT;
      auto q_rest = [&](auto ic) INL {
        constexpr int i = ic.v;
        if constexpr (L == 0 && i == 0) {
          OPV(csq[0]); OPV(csq[1]);
          csq[0] *= -1.f / 448.f; csq[1] *= -1.f / 448.f;
          lds_st<f32x4>(ca, csq[0]);
          lds_st<f32x4>(ca + 64, csq[1]);
        }
        if constexpr (i < 8) q_epi(ic);
        SB();
      };
      static_for<0, 8>([&](auto ic) INL {
        auto req = [&]() INL { ld16hc<(ic.v & 3) * 1024>(p[ic.v], rW, l16, so + (ic.v >> 2) * 4096); };
        if constexpr (ic.v < NQ) q_step(IC<Q1 + ic.v>{}, req);
        else { req(); SB(); q_rest(IC<ic.v - NQ>{}); }
      });
      VMW(12);
      if constexpr (NT) w_touch(1, 0);
      wru(IC<0>{}); wru(IC<1>{});
      unsigned a_kr = L_KVF + l16;
      OPV(a_kr);
      asm volatile("s_waitcnt lgkmcnt(0)" ::: "memory");
      SB();
      static_for<0, 8>([&](auto ic) INL {
        wdu(IC<4 + ic.v / 4>{}, IC<ic.v % 4>{});
        SB();
        if constexpr (ic.v < NQ) q_rest(IC<8 - NQ + ic.v>{});
        else if constexpr (ic.v == NQ) wo_prep();
        else if constexpr (L == 1) { wof_ld((ic.v - 1) >> 1, 2 * ((ic.v - 1) & 1)); wof_ld((ic.v - 1) >> 1, 2 * ((ic.v - 1) & 1) + 1); }
        SB();
      });
      v4u PQ[2] = {v4u{qp[0], qp[1], qp[2], qp[3]}, v4u{qp[4], qp[5], qp[6], qp[7]}};
      OPV(PQ[0]); OPV(PQ[1]);
      static_for<0, 8, 0>([&](auto ic) INL {
        ldw1<7 - ic.v + NBH>(p[ic.v]);
        if constexpr (ic.v == 0) ps = mfma_v(SEL, p[0]); else mfma_va(ps, SEL, p[ic.v]);
      });
      asm volatile("s_nop 7" : "+v"(ps));
      if (UNLIKELY(anynan(ps[0], ps[0]))) {
        REDO_COUNT(L * 6 + 0);
        ld_settled<8>(p, rW, l16, so, 1024);
        ps = Z4;
#pragma unroll
        for (int i = 0; i < 8; ++i) ps = mfma(SEL, p[i], ps);
        OPV(ps);
      }
      if constexpr (FB_AT == 0) st4c(vE, rW, a_fb, 0);
      lds_st<v2u>(a_kw, v2u{pk2(lo2(ps)), pk2(hi2(ps))});
      BAR();
      if constexpr (FB_AT == 1) st4c(vE, rW, a_fb, 0);
      const v4u ZZ = lds_ld<v4u>(a_z), KV0 = lds_ld<v4u>(a_kr), KV1 = lds_ld<v4u>(a_kr + 1024);
      SB();
      f32x4 dn[2], n0[2], n1_[2];
      dn[0] = mfma_v(ZZ, PQ[0]); dn[1] = mfma_v(ZZ, PQ[1]);
      n0[0] = mfma_v(KV0, PQ[0]); n1_[0] = mfma_v(KV1, PQ[0]);
      n0[1] = mfma_v(KV0, PQ[1]); n1_[1] = mfma_v(KV1, PQ[1]);
      asm volatile("s_nop 3" : "+v"(dn[0]), "+v"(dn[1]));
      float inv[2];
#pragma unroll
      for (int tl = 0; tl < 2; ++tl) { inv[tl] = __builtin_amdgcn_rcpf(dn[tl][0] + 1e-6f); OPV(inv[tl]); }
      SB();
#pragma unroll
      for (int tl = 0; tl < 2; ++tl) {
        OPV(n0[tl]); OPV(n1_[tl]);
        const f32x2 iv = bc(inv[tl]);
        st16c(v4u{pk2(lo2(n0[tl]) * iv), pk2(hi2(n0[tl]) * iv), padm & pk2(lo2(n1_[tl]) * iv), padm & pk2(hi2(n1_[tl]) * iv)}, rW, a_st, tl * 1024);
      }
      FENCE();
    }
  };
  auto back = [&](auto lc) INL {
    constexpr bool IDLE = false;
    LAYER_DEFS
    STAMP(6 + L * 20);
    FRESH();
    CODE_AHEAD();
    {
      const unsigned mbB = mb + HB * MB_HOP + c2 * MBS;
      v16u pkB;
      f32x4 acc[4];
      constexpr int WA = L ? 14 : 0, WB = L ? 3 : 5;
      wru(IC<2>{}); wru(IC<3>{});
      asm volatile("s_waitcnt lgkmcnt(8)" ::: "memory");
      static_for<0, 4>([&](auto ic) INL {
        constexpr int i = ic.v;
        wdu(IC<6>{}, ic);
        if constexpr (i == PKB) pkB = peek<16>(gws, mbB);
        SB();
#pragma unroll
        for (int x = WA + WB * i; x < WA + WB * (i + 1); ++x) wof_ld(x >> 2, x & 3);
        SB();
      });
#pragma unroll
      for (int c = 0; c < 4; ++c) acc[c] = up4(xr[c]);
      unsigned so = OFF_O + L * O_L + (T >> 1) * 32768 + (T & 1) * 1024, l16b = l16 + 4096;
      OPQ(so); OPV(l16b);
      v4u of[16];
      auto o_ld = [&](auto ic) INL { constexpr int i = ic.v; ld16hc<(i & 1) * 2048>(of[i], rW, (i & 2) ? l16b : l16, so + (i >> 2) * 8192); };
      FENCE();
      STAMP(7 + L * 20);
      poll<16>(pkB, gws, mbB, EE, 1, budget);
      STAMP(8 + L * 20);
      unsigned b_fc, ha[4], hr[2], sv0, sv1, c_fd;
      static_for<0, 10>([&](auto ic) INL {
        constexpr int i = ic.v;
        o_ld(ic);
        SB();
#pragma unroll
        for (int x = WA + 4 * WB + 3 * i; x < WA + 4 * WB + 3 * (i + 1) && x < 4 * WOP; ++x) wof_ld(x >> 2, x & 3);
        if constexpr (i == 6) { VMW(11); wru(IC<4>{}); }
        if constexpr (i == 8) wru(IC<5>{});
        if constexpr (i == 0) {
          LANE_COPY();
          b_fc = oob_unless(ln < 7, ln * (4 * MBS) + (mb + HC * MB_HOP + (t * 28 + w) * MBS + sl8(ds)));
          d_st = l16 + (oU + L * U_L);
          OPV(b_fc); OPV(d_st);
        } else if constexpr (i == 1) {
          LANE_COPY();
          sv0 = g * 128 + li * 8; sv1 = g == 3 ? OOB : sv0 + 512;
          c_fd = oob_unless(ln < 7, ln * (4 * MBS) + (mb + HD * MB_HOP + ((ds * 28 + 7 * w) * 4 + (t >> 3)) * MBS + sl8(t & 7)));
          OPV(sv0); OPV(sv1); OPV(c_fd);
        } else if constexpr (i == 2) {
          LANE_COPY();
          const unsigned X = (((li >> 1) & 3) << 2) | (li & 1) | ((li >> 3) << 1);
#pragma unroll
          for (int c = 0; c < 4; ++c) { ha[c] = L_HT + (16 * w + li) * 128 + (((4 * c + g) ^ X) * 8); OPV(ha[c]); }
        } else if constexpr (i == 4) {
          LANE_COPY();
          const unsigned rr = ln >> 2, XR_ = (((rr >> 1) & 3) << 2) | (rr & 1) | ((rr >> 3) << 1);
#pragma unroll
          for (int e = 0; e < 2; ++e) { hr[e] = L_HT + (32 * (w >> 1) + rr) * 128 + (((4 * (2 * (w & 1) + e) + (ln & 3)) ^ XR_) * 8); OPV(hr[e]); }
        }
        SB();
      });
      static_for<0, 16>([&](auto ic) INL {
        constexpr int hh = ic.v;
        ldw1<imin(15, hh + 9) - hh + (hh > 7 ? imin(hh - 7, 8) : 0) + (hh > FC_AT) + (L == 0 && (TW & 8) && hh > 6)>(of[hh]);
#pragma unroll
        for (int c = 0; c < 4; ++c) {
          acc[c] = mfma(wof[hh][c], of[hh], acc[c]);
          SB();
          if (c & 1) { const int x = 4 * WOP + 2 * hh + (c >> 1); if (x < 64) wof_ld(x >> 2, x & 3); }
          if (c == 0) {
            if constexpr (hh < 6) o_ld(IC<10 + hh>{});
            else if constexpr (hh == 6) { asm volatile("s_waitcnt lgkmcnt(8)" ::: "memory"); if constexpr (L == 0 && (TW & 8)) w_touch(1, 1); }
            else if constexpr (hh < 15) wdu(IC<7 + (hh - 7) / 4>{}, IC<(hh - 7) % 4>{});
          }
          if (c == 2 && hh == FC_AT) st4c(vE, rW, b_fc, 0);
          SB();
        }
      });
      v2u xn[4];
#pragma unroll
      for (int c = 0; c < 4; ++c) { OPV(acc[c]); xn[c] = v2u{pk2(lo2(acc[c])), pk2(hi2(acc[c]))}; OPV(xn[c]); }
      if (UNLIKELY(anynan(acc[0][0], acc[0][0]))) {
        REDO_COUNT(L * 6 + 1);
        ld_settled<16>(of, rW, l16, so, 2048);
#pragma unroll
        for (int c = 0; c < 4; ++c) acc[c] = up4(xr[c]);
#pragma unroll
        for (int x = 0; x < 64; ++x) { wof_ld(x >> 2, x & 3); acc[x & 3] = mfma(wof[x >> 2][x & 3], of[x >> 2], acc[x & 3]); SB(); }
#pragma unroll
        for (int c = 0; c < 4; ++c) { xn[c] = v2u{pk2(lo2(acc[c])), pk2(hi2(acc[c]))}; OPV(xn[c]); }
      }
#pragma unroll
      for (int c = 0; c < 4; ++c) xr[c] = xn[c];
      float su[2] = {0.f, 0.f}, sq[2] = {0.f, 0.f};
#pragma unroll
      for (int c = 0; c < 4; ++c) {
        su[0] = dot2(xr[c].x, 0x3f803f80u, su[0]); sq[0] = dot2(xr[c].x, xr[c].x, sq[0]);
        su[1] = dot2(xr[c].y, 0x3f803f80u, su[1]); sq[1] = dot2(xr[c].y, xr[c].y, sq[1]);
      }
      const float sum = xsum16_32(su[0] + su[1]), sqs = xsum16_32(sq[0] + sq[1]);
      st8c(v2u{asu(sum), umin_(asu(sqs), 0xfffffffeu)}, rW, b_st, 0);
      FENCE();
      STAMP(9 + L * 20);
      const unsigned mbC = mb + HC * MB_HOP + c2 * MBS;
      BAR();
      const v8u pkC = peek<7>(gws, mbC);
      static_for<0, 4>([&](auto ic) INL { wdu(IC<9>{}, ic); wr(IC<u0(L) + 6>{}, ic); SB(); });
      f32x2 x1[8];
#pragma unroll
      for (int c = 0; c < 4; ++c) { x1[2 * c] = up2(xr[c].x); x1[2 * c + 1] = up2(xr[c].y); OPV(x1[2 * c]); OPV(x1[2 * c + 1]); OPV(xr[c]); }
      unsigned sb = oS + L * STAT_L;
      OPQ(sb);
      FENCE();
      poll<7>(pkC, gws, mbC, EE, 1, budget);
      STAMP(10 + L * 20);
      v2u s0, s1;
      asm volatile("buffer_load_dwordx2 %0, %2, %4, %5 offen sc1\n\tbuffer_load_dwordx2 %1, %3, %4, %5 offen sc1"
                   : "=&v"(s0), "=&v"(s1) : "v"(sv0), "v"(sv1), "s"(rW), "s"(sb) : "memory");
      static_for<0, 8>([&](auto ic) INL { wdu(IC<10 + ic.v / 4>{}, IC<ic.v % 4>{}); });
      asm volatile("s_waitcnt vmcnt(8)" : "+v"(s0), "+v"(s1)::"memory");
      if (UNLIKELY(__builtin_amdgcn_ballot_w64(umax_(s0.y, s1.y) == ~0u) != 0)) poll_stat(rW, sv0, sv1, sb, s0, s1);
      if constexpr (FD_AT == 0) { asm volatile("s_barrier" ::: "memory"); st4c(vE, rW, c_fd, 0); }
      STAMP(11 + L * 20);
      const float mu = xsum16_32(asf(s0.x) + asf(s1.x)) * (1.f / 448.f);
      const float var = fmaxf(xsum16_32(asf(s0.y) + asf(s1.y)) * (1.f / 448.f) - mu * mu, 0.f);
      const float sc = __builtin_amdgcn_rsqf(var * (1.f + 1e-5f) + 1e-10f);
      const f32x2 sc2 = bc(sc), of2 = bc(-mu * sc);
#pragma unroll
      for (int c = 0; c < 4; ++c) lds_st<v2u>(ha[c], v2u{pk2(fma2(x1[2 * c], sc2, of2)), pk2(fma2(x1[2 * c + 1], sc2, of2))});
      BAR();
      const v4u h0 = trf(hr[0], hr[0] + 2048), h1 = trf(hr[1], hr[1] + 2048);
      if constexpr (FD_AT == 1) st4c(vE, rW, c_fd, 0);
      SB();
      st16c(h0, rW, c_st, 0);
      st16c(h1, rW, c_st, 1024);
      FENCE();
    }
    STAMP(12 + L * 20);
    FRESH();
    CODE_AHEAD();
    {
      const unsigned mbD = mb + HD * MB_HOP + c1 * MBS, mbE = mb + HE * MB_HOP + c2 * MBS;
      v8u pkD;
      asm volatile("s_barrier" ::: "memory");
      static_for<0, 8>([&](auto ic) INL {
        constexpr int i = ic.v;
        if constexpr (i == 4) asm volatile("s_waitcnt lgkmcnt(8)" ::: "memory");
        wdu(IC<12 + i / 4>{}, IC<i % 4>{});
        if constexpr (i == PKD) pkD = peek<8>(gws, mbD);
        if constexpr (i < 6) { wr(IC<u0(L) + 7 + i / 2>{}, IC<2 * (i & 1)>{}); wr(IC<u0(L) + 7 + i / 2>{}, IC<2 * (i & 1) + 1>{}); }
        SB();
      });
      unsigned hs = OFF_H + L * H_L + (ds1 * 64 + 16 * w) * 4096, us = OFF_U + L * U_L + (ds * 56 + 14 * w) * 4096;
      unsigned hv0 = l16 + ((w >> 1) ^ 1) * 1024, hv1 = hv0 ^ 1024;
      OPQ(hs); OPQ(us); OPV(hv0); OPV(hv1);
      FENCE();
      poll<8>(pkD, gws, mbD, EE, n1, budget);
      STAMP(13 + L * 20);
      constexpr int RS = RD + 5;
      v4u A[RS];
      f32x4 acc[4][2][4], bs[2], pp_[6];
      f32x2 s[4];
      v16u pkV;
      v8u pkE;
      unsigned rw[3], rb, d_fe, f_ff = 0, f_s1 = 0, f_xa = 0, f_xw = 0;
      float b2;
      auto a_ld = [&](int i) INL {
        if (i < 64) A[i % RS] = ld16a(r1, (gc(i) ? hv1 : hv0) + gh(i) * 2048, hs + gk(i) * 4096);
        else if (i < 120) A[i % RS] = ld16a(rW, l16 + gc(i) * 2048 + (gk(i) & 1) * 1024, us + (gk(i) >> 1) * 8192 + gh(i) * 4096);
        else if (L == 0 && i < 134) x_ld(i - 120);
      };
      auto a_chk = [&](int i) INL {
        const v4u z = A[i % RS];
        if (UNLIKELY(__builtin_amdgcn_ballot_w64(umax_(umax_(umax_(z.x, z.y), z.z), z.w) == ~0u) != 0)) {
          REDO_COUNT(L * 6 + 3);
          v4u y[1];
          if (i < 64) ld_settled<1>(y, r1, gc(i) ? hv1 : hv0, hs + gk(i) * 4096, 0);
          else ld_settled<1>(y, rW, l16, us + (gk(i) >> 1) * 8192 + gh(i) * 4096 + gc(i) * 2048 + (gk(i) & 1) * 1024, 0);
          A[i % RS] = y[0];
        }
        SB();
      };
      auto part_st = [&](int P, int y) INL {
        const f32x4 v = P < 2 ? (y < 2 ? acc[P][1][2 + y] : acc[P][0][y - 2]) : acc[P][y & 1][1 + (y >> 1)];
        lds_st<f32x4>(rw[y >> 1] + (y & 1) * 1024, v);
      };
      auto part_ld = [&](int P, int y) INL { pp_[y] = lds_ld<f32x4>(rb + y * 1024); };
      auto own = [&](int P, int y) INL -> f32x4& { return P < 2 ? acc[P][1][y] : acc[P][y][0]; };
      auto init2 = [&](int H, int x) INL {
        v2u c = xr[2 * H + x];
        OPV(c);
        f32x4 v = up4(c);
#pragma unroll
        for (int i = 0; i < 4; ++i) { float z = v[i] + b2; OPV(z); v[i] = z; }
        return v;
      };
      auto finish = [&](auto pc, auto&& extra) INL {
        constexpr int P = pc.v, H = P & 1;
#pragma unroll
        for (int y = 0; y < 2; ++y) {
          OPV(own(P, y));
          const f32x4 v = (own(P, y) + pp_[y]) + (pp_[2 + y] + pp_[4 + y]);
          s[2 * y] = lo2(v); s[2 * y + 1] = hi2(v);
          OPV(s[2 * y]); OPV(s[2 * y + 1]);
        }
        SB();
        extra(IC<0>{});
        if constexpr (P < 2) {
          if (UNLIKELY(anynan(s[0].x, s[0].x))) {
            REDO_COUNT(L * 6 + 2);
            f32x4 z[2] = {bs[0], bs[1]};
            LANE_COPY();
#pragma nounroll
            for (unsigned k = 0; k < 64; ++k) {
              v4u hf[1], wv[2];
              unsigned e[16];
#pragma unroll
              for (int x = 0; x < 16; ++x)
                e[x] = __builtin_amdgcn_raw_buffer_load_b16(rA, g * (4 * 3584) + li * 2 + (x >> 3) * 32,
                                                            (32 * k + 16 * ((x >> 2) & 1) + (x & 3)) * 3584 + fs * 128 + (w & 1) * 64, 0);
              ld_settled<1>(hf, r1, hv1 + H * 2048, OFF_H + L * H_L + (ds1 * 64 + k) * 4096, 0);
#pragma unroll
              for (int x = 0; x < 8; ++x) wv[x >> 2][x & 3] = (e[2 * x] & 0xffffu) | (e[2 * x + 1] << 16);
              z[0] = mfma(wv[0], hf[0], z[0]);
              z[1] = mfma(wv[1], hf[0], z[1]);
            }
#pragma unroll
            for (int y = 0; y < 4; ++y) { s[y] = half2(z[y >> 1], y & 1); OPV(s[y]); }
          }
          const float c0 = -2.f * 0.7978845608028654f * LOG2E;
          f32x2 y[4];
#pragma unroll
          for (int i = 0; i < 4; ++i) y[i] = s[i] * s[i];
#pragma unroll
          for (int i = 0; i < 4; ++i) y[i] = fma2(y[i], bc(c0 * 0.044715f), bc(c0));
#pragma unroll
          for (int i = 0; i < 4; ++i) y[i] = y[i] * s[i];
          SB();
          extra(IC<1>{});
#pragma unroll
          for (int i = 0; i < 4; ++i) y[i] = f32x2{__builtin_amdgcn_exp2f(y[i].x), __builtin_amdgcn_exp2f(y[i].y)};
          SB();
          extra(IC<2>{});
#pragma unroll
          for (int i = 0; i < 4; ++i) y[i] += bc(1.f);
#pragma unroll
          for (int i = 0; i < 4; ++i) y[i] = f32x2{__builtin_amdgcn_rcpf(y[i].x), __builtin_amdgcn_rcpf(y[i].y)};
          SB();
          extra(IC<3>{});
          st16c(v4u{pk2(s[0] * y[0]), pk2(s[1] * y[1]), pk2(s[2] * y[2]), pk2(s[3] * y[3])}, r1, d_st, H * 4096);
          st4c(vE, r1, d_fe + H * 128, 0);
        } else {
          const f32x2 pr = (s[0] + s[1]) + (s[2] + s[3]);
          if (UNLIKELY(anynan(pr.x, pr.y))) {
            REDO_COUNT(L * 6 + 5);
            f32x4 z[2] = {init2(H, 0), init2(H, 1)};
            LANE_COPY();
#pragma nounroll
            for (unsigned k = 0; k < 56; ++k) {
              v4u uf[2], wv;
              unsigned e[8];
#pragma unroll
              for (int x = 0; x < 8; ++x)
                e[x] = __builtin_amdgcn_raw_buffer_load_b16(rB, g * (4 * 4096) + li * 2, (32 * k + 16 * (x >> 2) + (x & 3)) * 4096 + T * 32, 0);
              ld_settled<2>(uf, rW, l16, OFF_U + L * U_L + (ds * 28 + (k >> 1)) * 8192 + H * 4096 + (k & 1) * 1024, 2048);
#pragma unroll
              for (int x = 0; x < 4; ++x) wv[x] = (e[2 * x] & 0xffffu) | (e[2 * x + 1] << 16);
              z[0] = mfma(uf[0], wv, z[0]);
              z[1] = mfma(uf[1], wv, z[1]);
            }
#pragma unroll
            for (int y = 0; y < 4; ++y) { s[y] = half2(z[y >> 1], y & 1); OPV(s[y]); }
          }
          xr[2 * H] = v2u{pk2(s[0]), pk2(s[1])};
          xr[2 * H + 1] = v2u{pk2(s[2]), pk2(s[3])};
          if constexpr (L == 0) {
            st16c(cat(xr[2 * H], xr[2 * H + 1]), rW, f_st, H * 1024);
            if (H == 0 || FF_AT == 1) st4c(vE, rW, f_ff + H * 128, 0);
          } else {
            lds_st<v2u>(f_xa ^ (H * 64), xr[2 * H]);
            lds_st<v2u>(f_xa ^ (H * 64 + 32), xr[2 * H + 1]);
          }
        }
        if constexpr (L == 0 || P == 0) FENCE(); else SB();
      };
#pragma unroll
      for (int i = 0; i < RD; ++i) {
        a_ld(i);
        SB();
        if (i == 0) {
          LANE_COPY();
          const unsigned a = L_B1 + (fs & 1) * 128 + (w & 1) * 64 + g * 8;
          bs[0] = up4(lds_ld<v2u>(a));
          bs[1] = up4(lds_ld<v2u>(a + 32));
          b2 = bflo(lds_ld<u16>(L_B2 + (T & 7) * 32 + li * 2));
          asm volatile("" : "+a"(bs[0]), "+a"(bs[1]), "+v"(b2));
        }
        if (i == 2) {
          LANE_COPY();
          const unsigned f7 = xcd >> 1;
          d_fe = oob_unless(ln < 8, ln * (28 * MBS) + (mb + HE * MB_HOP + ((8 * w * 7 + ds1) * 4 + f7) * MBS + sl8(fs - 7 * f7)));
          OPV(d_fe);
        }
        if (i == 4) {
#pragma unroll
          for (int c = 1; c < 4; ++c) { rw[c - 1] = L_R + ((w ^ c) * 3 + (c - 1)) * 2048 + l16; OPV(rw[c - 1]); }
          rb = L_R + w * 6144 + l16;
          OPV(rb);
        }
        SB();
      }
      if constexpr (CHKD > 0) a_chk(0);
      static_for<0, 120, L ? 63 : 120>([&](auto ic) INL {
        constexpr int i = ic.v, n = gn(i), kk = gk(i), x = gc(i), P = (i < 64 ? 0 : 2) + gh(i), NP = i < 64 ? 32 : 28;
        static_assert(cum(i) > 120 || cum(i) - 1 - RS < i, "ring overrun");
        static_assert(gh(i) == 1 || x == 1 || (i < 64 && w1u(L, kk) < 0) || WP[L].done[i < 64 ? w1u(L, kk) : nu1(L) + kk] < i, "weights late");
        constexpr int rd = WP[L].rd[i];
        auto wplace = [&](auto zc) INL {
          constexpr int k = WP[L].dma[i][zc.v];
          if constexpr (k >= 0) {
            if constexpr (k % 4 == 0) asm volatile("s_waitcnt lgkmcnt(8)" ::: "memory");
            wd(IC<k>{});
          }
        };
        const v4u cur = A[i % RS];
        auto mm = [&](auto yc) INL {
          constexpr int y = yc.v;
          if constexpr (i < 64 && res_k(kk)) {
            constexpr int a = (y + 2) & 3, z = RA0 + 16 * (kk - RK0) + 4 * a;
            asm volatile("v_mfma_f32_16x16x32_bf16 %0, a[%c2:%c3], %1, %0" : "+a"(acc[P][x][a]) : "v"(cur), "n"(z), "n"(z + 3));
          } else if constexpr (i < 64) { const int a = (y + 2) & 3; acc[P][x][a] = mfma(W[kk * 4 + a], cur, kk ? acc[P][x][a] : (x == 1 && a < 2) ? bs[a] : Z4); }
          else { const int b = (y + 1) & 3; acc[P][x][b] = mfma(cur, W2[kk * 4 + b], (kk || b == 0) ? acc[P][x][b] : Z4); }
          SB();
        };
        mm(IC<0>{});
        if constexpr (i == PE0 || i == PE1) poll<7>(pkE, gws, mbE + (i == PE1 ? 128 : 0), EE, 1, budget);
        if constexpr (L == 0 && i == PX0) poll<14>(pkV, gws, OFF_MB + HF * MB_HOP + cA * MBS, EE, 1, budget);
        if constexpr (cum(i) > cum0(i)) a_ld(cum0(i));
        else wplace(IC<0>{});
        SB();
        mm(IC<1>{});
        if constexpr (n >= NP - 4 + (P >= 2)) part_st(P, P < 2 ? 2 + n - (NP - 4) : 2 * (n - (NP - 3)));
        if constexpr (P > 0 && n == 0) { part_st(P - 1, P < 3 ? 0 : 1); part_st(P - 1, P < 3 ? 1 : 3); }
        if constexpr (P > 0 && n == 2) { part_ld(P - 1, 0); part_ld(P - 1, 1); }
        if constexpr (WP[L].wait[i]) asm volatile("s_waitcnt vmcnt(%0)" ::"n"(NY) : "memory");
        if constexpr (rd >= 0) wr(IC<rd / 2>{}, IC<2 * (rd & 1)>{});
        if constexpr (i == 47) STAMP(16 + L * 20);
        if constexpr (i == PE0 - PKE || i == PE1 - PKE) pkE = peek<7>(gws, mbE + (i > 64 ? 128 : 0));
        if constexpr (L == 0 && i == 16) pkE = peek<7>(gws, OFF_MB + HW2 * MB_HOP + gw * MBS + 128);
        if constexpr (L == 0 && i == 26) poll<7>(pkE, gws, OFF_MB + HW2 * MB_HOP + gw * MBS + 128, EE, 1, budget);
        if constexpr (L == 0 && i == 87) pkE = peek<7>(gws, OFF_MB + MB_L + HW2 * MB_HOP + gw * MBS);
        if constexpr (L == 0 && i == 97) poll<7>(pkE, gws, OFF_MB + MB_L + HW2 * MB_HOP + gw * MBS, EE, 1, budget);
        if constexpr (L == 0 && i == PX0 - 8) pkV = peek<14>(gws, OFF_MB + HF * MB_HOP + cA * MBS);
        if constexpr (L == 0 && i >= 106) wf_ld((i - 106) >> 1, 2 * (i & 1));
        SB();
        mm(IC<2>{});
        if constexpr (cum(i) > cum0(i) + 1) a_ld(cum0(i) + 1);
        else if constexpr (L == 0 && i >= IMG0 && i < IMG0 + 14) img_dma(i - IMG0);
        else wplace(IC<1>{});
        if constexpr (P == 3 && n == 0) part_st(2, 5);
        if constexpr (P > 0 && n == 2) { part_ld(P - 1, 2); part_ld(P - 1, 3); }
        if constexpr (i == 58 || i == 59 || i == 88 || i == 89) { acc[i < 64 ? 2 : 3][i & 1][0] = init2(i < 64 ? 0 : 1, i & 1); asm volatile("" : "+a"(acc[i < 64 ? 2 : 3][i & 1][0])); }
        if constexpr (L == 0 && i == 98) x_prep(1);
        if constexpr (L == 1 && i == 98) { f_s1 = (f_st ^ 64) + 8 * 896; f_xw = L_XS + w * 4096 + l16; OPV(f_s1); OPV(f_xw); }
        if constexpr (L == 0 && i >= 106) wf_ld((i - 106) >> 1, 2 * (i & 1) + 1);
        SB();
        if constexpr (i + 1 < CHKD || (i + 1 >= 64 && i + 1 < 64 + CHKE) || (i + 1 >= 92 && i + 1 < 92 + CHKE)) a_chk(i + 1);
        mm(IC<3>{});
        if constexpr (rd >= 0) wr(IC<rd / 2>{}, IC<2 * (rd & 1) + 1>{});
        if constexpr (i == 45 || (L == 1 && i == 75)) asm volatile("s_barrier" ::: "memory");
        if constexpr (L == 0 && i == IMG0 - 1) BAR();
        if constexpr (i == IMG0 + 18) { if constexpr (L == 0) VMW(4); asm volatile("s_barrier" ::: "memory"); }
        static_assert(L == 1 || (WP[0].last < IMG0 && IMG0 <= 89 && cum(IMG0) == cum0(IMG0) + 1), "");
        if constexpr (P > 0 && n == 1) BAR();
        if constexpr (P > 0 && n == 2) { part_ld(P - 1, 4); part_ld(P - 1, 5); }
        if constexpr (i == B0) finish(IC<0>{}, [&](auto kc) INL { a_ld(cum(B0) + kc.v); SB(); });
        if constexpr (i == 63) { STAMP(14 + L * 20); if constexpr (L == 0) CODE_AHEAD(); }
        if constexpr (i == 67) { finish(IC<1>{}, [&](auto kc) INL { wplace(IC<2 + kc.v>{}); SB(); }); STAMP(15 + L * 20); }
        if constexpr (i == 70) {
          LANE_COPY();
          if constexpr (L == 0) {
            f_ff = oob_unless(ln < 16, ln * (4 * MBS) + (OFF_MB + HF * MB_HOP + ((T >> 3) * 64 + ((T & 7) >> 1)) * MBS + (T & 1) * 64 + ds * 4));
            f_st = l16 + oX;
            OPV(f_ff); OPV(f_st);
          } else {
            f_st = xrow_off(ln, 0) + (16 * T * 896 + ds * 128);
            f_xa = L_XS + w * 4096 + (li >> 3) * 1024 + (li & 7) * 128 + (g & 1) * 8 + (((g >> 1) ^ (li >> 1)) * 16);
            OPV(f_st); OPV(f_xa);
          }
        }
        if constexpr (i == 95) finish(IC<2>{}, [&](auto) INL {});
        if constexpr (i == 119) {
          STAMP(17 + L * 20);
          part_st(3, 1); part_st(3, 3); part_st(3, 5);
          BAR();
          if constexpr (L == 0 && FF_AT == 0) st4c(vE, rW, f_ff + 128, 0);
#pragma unroll
          for (int y = 0; y < 6; ++y) part_ld(3, y);
          SB();
          finish(IC<3>{}, [&](auto) INL {});
        }
        SB();
      });
      if constexpr (L == 1) {
        const rsrc_t rOut = mk(gout, 2048 * 448 * 2);
        const v4u o0 = lds_ld<v4u>(f_xw), o1 = lds_ld<v4u>(f_xw + 1024);
        __builtin_amdgcn_raw_buffer_store_b128(o0, rOut, f_st, 0, 0);
        __builtin_amdgcn_raw_buffer_store_b128(o1, rOut, f_s1, 0, 0);
        st4c(vE, mk(gws, gw == 0 ? 4 : 0), l16, 0);
      } else FENCE();
      STAMP(18 + L * 20);
    }
    if constexpr (L == 0) { FRESH(); CODE_AHEAD(); }
  };
  front(IC<0>{}, IC<0>{});
  if (UNLIKELY(!act2)) {
      asm volatile("v_mbcnt_lo_u32_b32 %0, -1, 0\n\tv_mbcnt_hi_u32_b32 %0, -1, %0\n\tv_lshlrev_b32 %0, 4, %0\n\tv_mov_b32 %1, %3\n\tv_mov_b32 %2, 0"
                   : "=&v"(l16), "=&v"(vE), "=&v"(wv1) : "s"((unsigned)EE));
      auto warm = [&](auto lc) INL {
        constexpr int L = lc.v;
        constexpr unsigned mb = OFF_MB + L * MB_L;
        unsigned lane_ = l16 >> 4;
        OPV(lane_);
        const unsigned lane = lane_, k = r - 28;
        unsigned sink = 0;
        auto touch = [&](unsigned v, unsigned so) INL { sink ^= __builtin_amdgcn_raw_buffer_load_b32(rW, v, so, SC1); };
        auto marks = [&](int y) INL {
          ALLONES();
#pragma nounroll
          for (unsigned n = 0; n < 8; ++n) {
            ROLES((bid - 224) * 8 + n)
            const rsrc_t q1 = mk(gws, act1 ? WS_BYTES : 0), q2 = mk(gws, act2 ? WS_BYTES : 0);
            if (y == 2) {
              st8c(v2u{n1v, n1v}, rW, lane * 8, oKV + KVP_L);
              st8c(v2u{n1v, n1v}, q2, oob_unless(lane < 16, lane * 8), oS + STAT_L + ds * 128);
            }
#pragma unroll
            for (int e = 0; e < 2; ++e) {
              if (y == 2) st16c(N4, rW, l16, oO + O_L + e * 1024);
              if (y != 1) st16c(N4, q2, l16, oH + (y >> 1) * H_L + e * 1024);
              if (y != 0) st16c(N4, q1, l16, oU + (y >> 1) * U_L + e * 4096);
              if (y == 1) st16c(N4, q2, l16, oX + e * 1024);
            }
          }
          VMW(0);
          st4c(vE, rW, oob_unless(lane < 56, (lane & 7) * (4 * MBS) + sl8(lane >> 3)), OFF_MB + (y >> 1) * MB_L + HW2 * MB_HOP + ((bid - 224) * 32 + w) * MBS + (y & 1) * 128);
        };
        VMW(0);
        if constexpr (L == 0 && MKP == 0) marks(1);
#pragma unroll
        for (int y = (L == 0 && (TW & 8)) ? 7 : (L == 0 && (TW & 4)) ? 3 : 0; y < 7; ++y) sink ^= __builtin_amdgcn_raw_buffer_load_b32(rB, lane * 4096, (448 * w + 64 * y) * 4096 + (4 * xcd + k) * 128, 0);
        if constexpr (L == 0) STAMP(40);
        if constexpr (L == 0) { if (MKP == 1) marks(1); STAMP(41); }
        {
          const unsigned d = odd * 4 + k, on = 7 - d;
          WAITMB(8, mb + HD * MB_HOP + ((d * 28 + 7 * (xcd >> 1)) * 4 + w) * MBS, on);
          const unsigned v = on ? (lane >> 4) * 4096 + (lane & 15) * 128 : OOB;
#pragma unroll
          for (int y = 0; y < 4; ++y) touch(v, OFF_H + L * H_L + (d * 64 + 16 * w + 4 * y) * 4096 + 2048);
        }
        if constexpr (L == 0) marks(2);
#pragma unroll
        for (int H = 1 - L; H < 2; ++H)
#pragma unroll
          for (int z = 0; z < 2; ++z) {
            const unsigned d = k + 4 * z, on = 7 - d;
            WAITMB(7, mb + HE * MB_HOP + ((4 * xcd * 7 + d) * 4 + w) * MBS + H * 128, on);
#pragma unroll
            for (int y = 0; y < 4; ++y)
              touch((on && (y < 3 || lane < 32)) ? (lane >> 5) * 8192 + (lane & 31) * 128 : OOB, OFF_U + L * U_L + (d * 56 + 14 * w) * 4096 + y * 16384 + H * 4096);
          }
        asm volatile("" ::"v"(sink));
      };
      STAMP(39);
      warm(IC<0>{});
      STAMP(42);
      BAR();
#pragma unroll
      for (int u = 0; u < 14; ++u) img_dma(u);
      VMW(0);
      BAR();
      x_prep(1);
#pragma unroll
      for (int x = 0; x < 4 * WFP; ++x) wf_ld(x >> 2, x & 3);
      WAITMB(7, OFF_MB + MB_L + HW2 * MB_HOP + gw * MBS, 1);
      WAITMB(14, OFF_MB + HF * MB_HOP + cA * MBS, 1);
#pragma unroll
      for (int n = 0; n < 14; ++n) x_ld(n);
      front(IC<1>{}, IC<1>{});
      warm(IC<1>{});
      __builtin_amdgcn_endpgm();
  }
  back(IC<0>{});
  front(IC<1>{}, IC<0>{});
  back(IC<1>{});
}
#undef T
#undef c1
#undef c2

extern "C" void kernel_launch(void* const* d_in, const int* in_sizes, int n_in, void* d_out, int out_size, void* d_ws, size_t ws_size,
                              hipStream_t stream) {
  fused_kernel<<<256, 256, 0, stream>>>((const u16*)d_in[0], (const u16*)d_in[1], (const u16*)d_in[2], (const u16*)d_in[4],
                                        (const u16*)d_in[5], (const u16*)d_in[6], (const u16*)d_in[7], (u16*)d_out, (char*)d_ws);
}
```
